```python
import torch

def _softmax(x, axis=-1):
    return torch.softmax(x, dim=axis)

def _gelu(x):
    return torch.nn.functional.gelu(x, approximate='tanh')

def _silu(x):
    return torch.nn.functional.silu(x)

def _sigmoid(x):
    return torch.sigmoid(x)

def _layernorm(x, axis=-1, eps=1e-05):
    m = x.mean(dim=axis, keepdim=True)
    v = x.var(dim=axis, keepdim=True, unbiased=False)
    return (x - m) / torch.sqrt(v + eps)

def _scatter_add(tgt, idx, src, axis=0):
    return tgt.clone().index_add_(axis, idx, src)

def _segment_sum(src, seg, n):
    out = torch.zeros((n,) + src.shape[1:], dtype=src.dtype)
    return out.index_add_(0, seg, src)

def _pad(x, pw):
    flat = tuple((v for pair in reversed(pw) for v in pair))
    return torch.nn.functional.pad(x, flat)
kernel_name = 'adaLN_cond__linear_kernel_hedgehog__glu_gated_geglu__prompt'

def setup_inputs():

    _w = lambda a: (lambda t: t.to(torch.bfloat16) if t.is_floating_point() else t)(torch.as_tensor(a))

    def _A_setup():
        B, S, D, H = (1, 2048, 512, 32)
        Dh = D // H
        return {'x': _w((lambda *s: torch.randn(*s, dtype=torch.bfloat16) * 0.1)(B, S, D)), 'wqkv': _w((lambda *s: torch.randn(*s, dtype=torch.bfloat16) * 0.2)(D, 3 * D)), 'w_phi': _w((lambda *s: torch.randn(*s, dtype=torch.bfloat16) * 0.2)(H, Dh, Dh) * Dh ** (-0.5)), 'wo': _w((lambda *s: torch.randn(*s, dtype=torch.bfloat16) * 0.2)(D, D))}
    _A_in = _A_setup()

    def _B_setup():
        B, S, Dm, Df = (1, 2048, 512, 1376)
        return {'x': _w((lambda *s: torch.randn(*s, dtype=torch.bfloat16) * 0.1)(B, S, Dm)), 'w_gate': _w((lambda *s: torch.randn(*s, dtype=torch.bfloat16) * 3.2)(Dm, Df) * Dm ** (-0.5)), 'w_up': _w((lambda *s: torch.randn(*s, dtype=torch.bfloat16) * 3.2)(Dm, Df) * Dm ** (-0.5)), 'w_down': _w((lambda *s: torch.randn(*s, dtype=torch.bfloat16) * 3.2)(Df, Dm) * Df ** (-0.5))}
    _B_in = _B_setup()
    return {'A_x': _A_in['x'], 'A_wqkv': _A_in['wqkv'], 'A_w_phi': _A_in['w_phi'], 'A_wo': _A_in['wo'], 'B_x': _B_in['x'], 'B_w_gate': _B_in['w_gate'], 'B_w_up': _B_in['w_up'], 'B_w_down': _B_in['w_down'], 'c': _w((lambda *s: torch.randn(*s, dtype=torch.bfloat16) * 0.4)(1, 512) * 8.0), 'w_mod': _w((lambda *s: torch.randn(*s, dtype=torch.bfloat16) * 0.4)(512, 6 * 512) * 512 ** (-0.5) * 8.0)}

def reference(A_x, A_wqkv, A_w_phi, A_wo, B_x, B_w_gate, B_w_up, B_w_down, c, w_mod):

    def _A_ref(x, wqkv, w_phi, wo):
        B, S, D = x.shape
        H = 32
        Dh = D // H
        qkv = (lambda x, *d: x.permute(*d))((x @ wqkv).reshape(B, S, 3, H, Dh), 2, 0, 3, 1, 4)
        q, k, v = (qkv[0], qkv[1], qkv[2])
        pq = torch.einsum('bhsd,hdf->bhsf', q, w_phi)
        pk = torch.einsum('bhsd,hdf->bhsf', k, w_phi)
        phi_q = torch.concatenate([_softmax(pq, axis=-1), _softmax(-pq, axis=-1)], axis=-1)
        phi_k = torch.concatenate([_softmax(pk, axis=-1), _softmax(-pk, axis=-1)], axis=-1)
        kv = torch.einsum('bhsf,bhsd->bhfd', phi_k, v)
        z = torch.einsum('bhsf->bhf', phi_k)
        num = torch.einsum('bhqf,bhfd->bhqd', phi_q, kv)
        den = torch.einsum('bhqf,bhf->bhq', phi_q, z)[..., None] + 1e-06
        o = num / den
        return (lambda x, *d: x.permute(*d))(o, 0, 2, 1, 3).reshape(B, S, D) @ wo

    def _B_ref(x, w_gate, w_up, w_down):
        g = torch.einsum('bsd,df->bsf', x, w_gate)
        u = torch.einsum('bsd,df->bsf', x, w_up)
        h = _gelu(g) * u
        return torch.einsum('bsf,fd->bsd', h, w_down)
    x = A_x
    mod = (_silu(c) @ w_mod).reshape(c.shape[0], 6, 1, -1)
    sa, ca, ga, sb, cb, gb = (mod[:, 0].clone(), mod[:, 1].clone(), mod[:, 2].clone(), mod[:, 3].clone(), mod[:, 4].clone(), mod[:, 5].clone())
    h = _layernorm(x, axis=-1) * (1 + ca) + sa
    x = x + ga * _A_ref(h, A_wqkv, A_w_phi, A_wo)
    h = _layernorm(x, axis=-1) * (1 + cb) + sb
    x = x + gb * _B_ref(h, B_w_gate, B_w_up, B_w_down)
    return x
```

```cpp
#include <hip/hip_runtime.h>
#include <hip/hip_bf16.h>
#include <stdint.h>

typedef short bf16x8 __attribute__((ext_vector_type(8)));
typedef short bf16x4 __attribute__((ext_vector_type(4)));
typedef float f32x4 __attribute__((ext_vector_type(4)));
typedef float f32x2 __attribute__((ext_vector_type(2)));
typedef unsigned u32x4 __attribute__((ext_vector_type(4)));
typedef unsigned u32x2 __attribute__((ext_vector_type(2)));
typedef __bf16 bf16x2_t __attribute__((ext_vector_type(2)));
typedef unsigned short u16;

#define DI __device__ __forceinline__
#define MFMA32(a, b, c) __builtin_amdgcn_mfma_f32_16x16x32_bf16(__builtin_bit_cast(bf16x8, (a)), __builtin_bit_cast(bf16x8, (b)), (c), 0, 0, 0)

constexpr int S_ = 2048, D_ = 512, H_ = 32, F_ = 1376;
constexpr int NMF = S_ / 16;
constexpr int KS_D = D_ / 32;
constexpr int KS_F = F_ / 32;
constexpr int NPAIR = F_ / 16;
constexpr float LOG2E = 1.4426950408889634f;

constexpr size_t OFF_MOD  = 0;
constexpr size_t OFF_KV   = 16384;
constexpr size_t OFF_Z    = OFF_KV + 65536;
constexpr size_t OFF_XN   = OFF_Z + 4096;
constexpr size_t OFF_WQKV = OFF_XN + (size_t)S_ * D_ * 2;
constexpr size_t OFF_WO   = OFF_WQKV + (size_t)96 * 16384;
constexpr size_t OFF_WGU  = OFF_WO + (size_t)32 * 16384;
constexpr size_t OFF_WD   = OFF_WGU + (size_t)88 * 2 * 16384;
constexpr size_t OFF_PHIQ = OFF_WD + (size_t)32 * KS_F * 1024;
constexpr size_t OFF_X1   = OFF_PHIQ + (size_t)NMF * 32 * 1024;
constexpr size_t OFF_ST   = OFF_X1 + (size_t)S_ * D_ * 2;
constexpr size_t OFF_HH   = OFF_ST + (size_t)S_ * 8 * 8;

DI unsigned pk_bf16(float a, float b) {
    f32x2 v = {a, b};
    bf16x2_t r = __builtin_convertvector(v, bf16x2_t);
    return __builtin_bit_cast(unsigned, r);
}
DI float bf_lo(unsigned u) { return __uint_as_float(u << 16); }
DI float bf_hi(unsigned u) { return __uint_as_float(u & 0xffff0000u); }
DI float bf_round(float a) { return bf_lo(pk_bf16(a, 0.f)); }
DI float fexp2(float x) { return __builtin_amdgcn_exp2f(x); }
DI float frcp(float x) { return __builtin_amdgcn_rcpf(x); }

constexpr int K0_MOD = 96;
constexpr int K0_XN = 128;
constexpr int K0_FOLD = 64;
constexpr int K0_WV = 16, K0_WO = 16, K0_WG = 43, K0_WU = 43, K0_WD = 43;
constexpr int K0_B0 = K0_MOD, K0_B1 = K0_B0 + K0_XN, K0_B2 = K0_B1 + K0_FOLD, K0_B3 = K0_B2 + K0_WV, K0_B4 = K0_B3 + K0_WO,
              K0_B5 = K0_B4 + K0_WG, K0_B6 = K0_B5 + K0_WU, K0_B7 = K0_B6 + K0_WD;
constexpr int K0_GRID = K0_B7;

template <int KMAP, int NMAP>
DI void pretile_thread(const u16* __restrict__ W, int ldw, int kc  , int cg  ,
                       u32x4* __restrict__ dst, int nks, int nf_mul, int nf_add) {
    const int ks = kc >> 2, g = kc & 3;
    u32x4 r[8];
#pragma unroll
    for (int j = 0; j < 8; ++j) {
        const int k = ks * 32 + (KMAP == 0 ? 8 * g + j : 16 * (j >> 2) + 4 * g + (j & 3));
        r[j] = *(const u32x4*)(W + (size_t)k * ldw + cg * 8);
    }
#pragma unroll
    for (int c = 0; c < 8; ++c) {
        u32x4 o;
#pragma unroll
        for (int jj = 0; jj < 4; ++jj) {
            const unsigned a = r[2 * jj][c >> 1], b = r[2 * jj + 1][c >> 1];
            o[jj] = (c & 1) ? ((a >> 16) | (b & 0xffff0000u)) : ((a & 0xffffu) | (b << 16));
        }
        const int n = cg * 8 + c;
        int nf, rr;
        if (NMAP == 0) { nf = n >> 4; rr = n & 15; }
        else { const int cc = n & 31; nf = 2 * (n >> 5) + ((cc >> 2) & 1); rr = 4 * (cc >> 3) + (cc & 3); }
        dst[((size_t)(nf * nf_mul + nf_add) * nks + ks) * 64 + g * 16 + rr] = o;
    }
}

__global__ __launch_bounds__(256) void k0_prep(const u16* __restrict__ x, const u16* __restrict__ wqkv, const u16* __restrict__ wphi,
                                               const u16* __restrict__ wo, const u16* __restrict__ wg, const u16* __restrict__ wu,
                                               const u16* __restrict__ wd, const u16* __restrict__ cvec, const u16* __restrict__ wmod,
                                               char* __restrict__ ws) {
    __shared__ float red[64 * 33];
    const int b = blockIdx.x, t = threadIdx.x;
    if (b < K0_B0) {
        const int cgp = t & 3, kg = t >> 2;
        float acc[8];
#pragma unroll
        for (int i = 0; i < 8; ++i) acc[i] = 0.f;
        u32x4 wv[8]; float sc[8];
#pragma unroll
        for (int i = 0; i < 8; ++i) {
            const int k = kg + 64 * i;
            wv[i] = *(const u32x4*)(wmod + (size_t)k * 3072 + b * 32 + cgp * 8);
            const float c = bf_lo(cvec[k]);
            sc[i] = bf_round(c / (1.f + __expf(-c)));
        }
#pragma unroll
        for (int i = 0; i < 8; ++i) {
#pragma unroll
            for (int j = 0; j < 4; ++j) {
                acc[2 * j] += sc[i] * bf_lo(wv[i][j]);
                acc[2 * j + 1] += sc[i] * bf_hi(wv[i][j]);
            }
        }
#pragma unroll
        for (int j = 0; j < 8; ++j) red[kg * 33 + cgp * 8 + j] = acc[j];
        __syncthreads();
        if (t < 32) {
            float s = 0.f;
            for (int k = 0; k < 64; ++k) s += red[k * 33 + t];
            ((float*)(ws + OFF_MOD))[b * 32 + t] = bf_round(s);
        }
        if (b < 68) ((float*)(ws + OFF_KV))[b * 256 + t] = 0.f;
    } else if (b < K0_B1) {
        const int mf = b - K0_B0, r = t & 15, c = t >> 4;
        const u16* xr = x + (size_t)(mf * 16 + r) * D_;
        u32x4 v[4];
#pragma unroll
        for (int i = 0; i < 4; ++i) v[i] = *(const u32x4*)(xr + (c + 16 * i) * 8);
        float f[32];
        float s = 0.f;
#pragma unroll
        for (int i = 0; i < 4; ++i)
#pragma unroll
            for (int j = 0; j < 4; ++j) { f[i * 8 + 2 * j] = bf_lo(v[i][j]); f[i * 8 + 2 * j + 1] = bf_hi(v[i][j]); s += f[i * 8 + 2 * j] + f[i * 8 + 2 * j + 1]; }
        red[c * 16 + r] = s;
        __syncthreads();
        float m = 0.f;
#pragma unroll
        for (int i = 0; i < 16; ++i) m += red[i * 16 + r];
        m *= (1.f / D_);
        float q = 0.f;
#pragma unroll
        for (int i = 0; i < 32; ++i) { f[i] -= m; q += f[i] * f[i]; }
        red[512 + c * 16 + r] = q;
        __syncthreads();
        float var = 0.f;
#pragma unroll
        for (int i = 0; i < 16; ++i) var += red[512 + i * 16 + r];
        const float rs = rsqrtf(var * (1.f / D_) + 1e-5f);
        u32x4* dst = (u32x4*)(ws + OFF_XN) + (size_t)mf * 16 * 64;
#pragma unroll
        for (int i = 0; i < 4; ++i) {
            u32x4 o;
#pragma unroll
            for (int j = 0; j < 4; ++j) o[j] = pk_bf16(f[i * 8 + 2 * j] * rs, f[i * 8 + 2 * j + 1] * rs);
            const int ch = c + 16 * i;
            dst[(ch >> 2) * 64 + (ch & 3) * 16 + r] = o;
        }
    } else if (b < K0_B2) {
        const int wv = (b - K0_B1) * 4 + (t >> 6);
        const int hq = wv >> 2, kq = wv & 3;
        const int l = t & 63, r = l & 15, g = l >> 4, h = hq & 31;
        bf16x4 bphi;
#pragma unroll
        for (int j = 0; j < 4; ++j) bphi[j] = (short)wphi[h * 256 + (4 * g + j) * 16 + r];
        char* dst = ws + OFF_WQKV + (size_t)hq * 16384;
#pragma unroll
        for (int i = 0; i < 8; ++i) {
            const int rb = kq * 8 + i;
            const bf16x4 a = *(const bf16x4*)(wqkv + (size_t)(rb * 16 + r) * 1536 + hq * 16 + 4 * g);
            f32x4 d = {0.f, 0.f, 0.f, 0.f};
            d = __builtin_amdgcn_mfma_f32_16x16x16bf16_1k(a, bphi, d, 0, 0, 0);
            u32x2 o;
            o[0] = pk_bf16(d[0] * LOG2E, d[1] * LOG2E);
            o[1] = pk_bf16(d[2] * LOG2E, d[3] * LOG2E);
            const int ks = rb >> 1, gp = 2 * (rb & 1) + (g >> 1);
            *(u32x2*)(dst + ks * 1024 + (gp * 16 + r) * 16 + (g & 1) * 8) = o;
        }
    } else if (b < K0_B3) {
        const int id = (b - K0_B2) * 256 + t;
        pretile_thread<0, 0>(wqkv + 1024, 1536, id >> 6, id & 63, (u32x4*)(ws + OFF_WQKV), KS_D, 1, 64);
    } else if (b < K0_B4) {
        const int id = (b - K0_B3) * 256 + t;
        pretile_thread<1, 1>(wo, 512, id >> 6, id & 63, (u32x4*)(ws + OFF_WO), KS_D, 1, 0);
    } else if (b < K0_B5) {
        const int id = (b - K0_B4) * 256 + t;
        pretile_thread<0, 0>(wg, F_, id / 172, id % 172, (u32x4*)(ws + OFF_WGU), KS_D, 2, 0);
    } else if (b < K0_B6) {
        const int id = (b - K0_B5) * 256 + t;
        pretile_thread<0, 0>(wu, F_, id / 172, id % 172, (u32x4*)(ws + OFF_WGU), KS_D, 2, 1);
    } else {
        const int id = (b - K0_B6) * 256 + t;
        pretile_thread<1, 1>(wd, 512, id >> 6, id & 63, (u32x4*)(ws + OFF_WD), KS_F, 1, 0);
    }
}

DI void softmax_pm(const f32x4 p, unsigned& o0, unsigned& o1, unsigned& o2, unsigned& o3) {
    float mx = fmaxf(fmaxf(p[0], p[1]), fmaxf(p[2], p[3]));
    float mn = fminf(fminf(p[0], p[1]), fminf(p[2], p[3]));
    mx = fmaxf(mx, __shfl_xor(mx, 16)); mx = fmaxf(mx, __shfl_xor(mx, 32));
    mn = fminf(mn, __shfl_xor(mn, 16)); mn = fminf(mn, __shfl_xor(mn, 32));
    float ep[4], en[4];
#pragma unroll
    for (int i = 0; i < 4; ++i) { ep[i] = fexp2(p[i] - mx); en[i] = fexp2(mn - p[i]); }
    float sp = (ep[0] + ep[1]) + (ep[2] + ep[3]);
    float sn = (en[0] + en[1]) + (en[2] + en[3]);
    sp += __shfl_xor(sp, 16); sp += __shfl_xor(sp, 32);
    sn += __shfl_xor(sn, 16); sn += __shfl_xor(sn, 32);
    const float rp = frcp(sp), rn = frcp(sn);
    o0 = pk_bf16(ep[0] * rp, ep[1] * rp); o1 = pk_bf16(ep[2] * rp, ep[3] * rp);
    o2 = pk_bf16(en[0] * rn, en[1] * rn); o3 = pk_bf16(en[2] * rn, en[3] * rn);
}

DI u32x2 tr_read(unsigned lds_addr) {
    u32x2 r;
    asm volatile("ds_read_b64_tr_b16 %0, %1\n\ts_waitcnt lgkmcnt(0)" : "=&v"(r) : "v"(lds_addr) : "memory");
    return r;
}

__global__ __launch_bounds__(256) void k1_qkv(char* __restrict__ ws) {
    __shared__ __attribute__((aligned(16))) u32x4 sA[4 * 16 * 64];
    __shared__ __attribute__((aligned(16))) char sP[4 * 4096];
    const int mt = blockIdx.x >> 3, hg = blockIdx.x & 7;
    const int w = threadIdx.x >> 6, l = threadIdx.x & 63, g = l >> 4;
    const int h = hg * 4 + w;
    const u32x4* WT = (const u32x4*)(ws + OFF_WQKV);
    const u32x4* XN = (const u32x4*)(ws + OFF_XN);
    const float* MOD = (const float*)(ws + OFF_MOD);

    u32x4 xa[4][4];
#pragma unroll
    for (int kk = 0; kk < 4; ++kk)
#pragma unroll
        for (int mf = 0; mf < 4; ++mf) xa[kk][mf] = XN[((size_t)(mt * 4 + mf) * 16 + (4 * w + kk)) * 64 + l];
    u32x4 bq[16], bk[16], bv[16];
#pragma unroll
    for (int ks = 0; ks < 16; ++ks) {
        bq[ks] = WT[((size_t)h * 16 + ks) * 64 + l];
        bk[ks] = WT[((size_t)(32 + h) * 16 + ks) * 64 + l];
        bv[ks] = WT[((size_t)(64 + h) * 16 + ks) * 64 + l];
    }
#pragma unroll
    for (int kk = 0; kk < 4; ++kk) {
        const int ks = 4 * w + kk;
        const int k0 = ks * 32 + 8 * g;
        const f32x4 s0 = *(const f32x4*)(MOD + k0), s1 = *(const f32x4*)(MOD + k0 + 4);
        const f32x4 c0 = *(const f32x4*)(MOD + 512 + k0), c1 = *(const f32x4*)(MOD + 512 + k0 + 4);
        float gg[8], ss[8];
#pragma unroll
        for (int j = 0; j < 4; ++j) { gg[j] = bf_round(1.f + c0[j]); gg[4 + j] = bf_round(1.f + c1[j]); ss[j] = s0[j]; ss[4 + j] = s1[j]; }
#pragma unroll
        for (int mf = 0; mf < 4; ++mf) {
            u32x4 o;
#pragma unroll
            for (int j = 0; j < 4; ++j)
                o[j] = pk_bf16(bf_lo(xa[kk][mf][j]) * gg[2 * j] + ss[2 * j], bf_hi(xa[kk][mf][j]) * gg[2 * j + 1] + ss[2 * j + 1]);
            sA[(mf * 16 + ks) * 64 + l] = o;
        }
    }
    __syncthreads();
    f32x4 aq[4], ak[4], av[4];
#pragma unroll
    for (int mf = 0; mf < 4; ++mf) { aq[mf] = (f32x4){0.f, 0.f, 0.f, 0.f}; ak[mf] = aq[mf]; av[mf] = aq[mf]; }
#pragma unroll
    for (int ks = 0; ks < 16; ++ks) {
#pragma unroll
        for (int mf = 0; mf < 4; ++mf) {
            const u32x4 a = sA[(mf * 16 + ks) * 64 + l];
            aq[mf] = MFMA32(bq[ks], a, aq[mf]);
            ak[mf] = MFMA32(bk[ks], a, ak[mf]);
            av[mf] = MFMA32(a, bv[ks], av[mf]);
        }
    }
    char* myP = sP + w * 4096;
#pragma unroll
    for (int mf = 0; mf < 4; ++mf) {
        unsigned o0, o1, o2, o3;
        softmax_pm(ak[mf], o0, o1, o2, o3);
        char* row = myP + (mf * 16 + (l & 15)) * 64;
        *(u32x2*)(row + g * 8) = (u32x2){o0, o1};
        *(u32x2*)(row + 32 + g * 8) = (u32x2){o2, o3};
    }
    asm volatile("s_waitcnt lgkmcnt(0)" ::: "memory");
    {
        const unsigned base = (unsigned)(size_t)myP;
        const int q4 = (l & 15) >> 2, p4 = l & 3;
        f32x4 kv[2], zz[2];
        kv[0] = (f32x4){0.f, 0.f, 0.f, 0.f}; kv[1] = kv[0]; zz[0] = kv[0]; zz[1] = kv[0];
        const u32x4 ones = {0x3f803f80u, 0x3f803f80u, 0x3f803f80u, 0x3f803f80u};
#pragma unroll
        for (int s = 0; s < 2; ++s) {
            u32x4 vb;
            vb[0] = pk_bf16(av[2 * s][0], av[2 * s][1]); vb[1] = pk_bf16(av[2 * s][2], av[2 * s][3]);
            vb[2] = pk_bf16(av[2 * s + 1][0], av[2 * s + 1][1]); vb[3] = pk_bf16(av[2 * s + 1][2], av[2 * s + 1][3]);
#pragma unroll
            for (int half = 0; half < 2; ++half) {
                const u32x2 lo = tr_read(base + (32 * s + 4 * g + q4) * 64 + half * 32 + p4 * 8);
                const u32x2 hi = tr_read(base + (32 * s + 16 + 4 * g + q4) * 64 + half * 32 + p4 * 8);
                const u32x4 pa = {lo[0], lo[1], hi[0], hi[1]};
                kv[half] = MFMA32(pa, vb, kv[half]);
                zz[half] = MFMA32(pa, ones, zz[half]);
            }
        }
        float* KV = (float*)(ws + OFF_KV) + (size_t)h * 512;
        float* Z = (float*)(ws + OFF_Z) + (size_t)h * 32;
#pragma unroll
        for (int half = 0; half < 2; ++half)
#pragma unroll
            for (int rg = 0; rg < 4; ++rg) {
                atomicAdd(KV + (half * 4 + rg) * 64 + l, kv[half][rg]);
                if ((l & 15) == 0) atomicAdd(Z + g * 8 + half * 4 + rg, zz[half][rg]);
            }
    }
    u32x4* PH = (u32x4*)(ws + OFF_PHIQ);
#pragma unroll
    for (int mf = 0; mf < 4; ++mf) {
        u32x4 o;
        unsigned o0, o1, o2, o3;
        softmax_pm(aq[mf], o0, o1, o2, o3);
        o[0] = o0; o[1] = o1; o[2] = o2; o[3] = o3;
        PH[((size_t)(mt * 4 + mf) * 32 + h) * 64 + l] = o;
    }
}

__global__ __launch_bounds__(256) void k2_wo(const u16* __restrict__ x, char* __restrict__ ws) {
    __shared__ __attribute__((aligned(16))) f32x4 sR[4 * 16 * 64];
    const int mt = blockIdx.x >> 3, nt = blockIdx.x & 7;
    const int w = threadIdx.x >> 6, l = threadIdx.x & 63, g = l >> 4;
    const u32x4* PH = (const u32x4*)(ws + OFF_PHIQ);
    const u32x4* WO = (const u32x4*)(ws + OFF_WO);
    const float* KV = (const float*)(ws + OFF_KV);
    const float* Z = (const float*)(ws + OFF_Z);
    const float* MOD = (const float*)(ws + OFF_MOD);

    f32x4 acc[4][4];
#pragma unroll
    for (int a = 0; a < 4; ++a)
#pragma unroll
        for (int b = 0; b < 4; ++b) acc[a][b] = (f32x4){0.f, 0.f, 0.f, 0.f};
#pragma unroll
    for (int hp = 0; hp < 4; ++hp) {
        const int ks = w * 4 + hp;
        u32x4 wf[4];
#pragma unroll
        for (int nf = 0; nf < 4; ++nf) wf[nf] = WO[((size_t)(nt * 4 + nf) * 16 + ks) * 64 + l];
        u32x4 of[4];
#pragma unroll
        for (int hh = 0; hh < 2; ++hh) {
            const int h = 2 * ks + hh;
            float kvv[8];
#pragma unroll
            for (int i = 0; i < 8; ++i) kvv[i] = KV[(size_t)h * 512 + i * 64 + l];
            const f32x4 z0 = *(const f32x4*)(Z + h * 32 + g * 8), z1 = *(const f32x4*)(Z + h * 32 + g * 8 + 4);
            u32x4 ka, za;
#pragma unroll
            for (int i = 0; i < 4; ++i) ka[i] = pk_bf16(kvv[2 * i], kvv[2 * i + 1]);
            za[0] = pk_bf16(z0[0], z0[1]); za[1] = pk_bf16(z0[2], z0[3]); za[2] = pk_bf16(z1[0], z1[1]); za[3] = pk_bf16(z1[2], z1[3]);
#pragma unroll
            for (int mf = 0; mf < 4; ++mf) {
                const u32x4 ph = PH[((size_t)(mt * 4 + mf) * 32 + h) * 64 + l];
                const f32x4 zero = {0.f, 0.f, 0.f, 0.f};
                const f32x4 num = MFMA32(ka, ph, zero);
                const f32x4 den = MFMA32(za, ph, zero);
                const float rd = 1.0f / (den[0] + 1e-6f);
                of[mf][2 * hh] = pk_bf16(num[0] * rd, num[1] * rd);
                of[mf][2 * hh + 1] = pk_bf16(num[2] * rd, num[3] * rd);
            }
        }
#pragma unroll
        for (int nf = 0; nf < 4; ++nf)
#pragma unroll
            for (int mf = 0; mf < 4; ++mf) acc[nf][mf] = MFMA32(wf[nf], of[mf], acc[nf][mf]);
    }
#pragma unroll
    for (int nf = 0; nf < 4; ++nf)
#pragma unroll
        for (int mf = 0; mf < 4; ++mf) sR[(w * 16 + nf * 4 + mf) * 64 + l] = acc[nf][mf];
    __syncthreads();
    const int mf = w;
    const int tok = (mt * 4 + mf) * 16 + (l & 15);
    float vals[16];
    u32x4* X1 = (u32x4*)(ws + OFF_X1);
#pragma unroll
    for (int p = 0; p < 2; ++p) {
        f32x4 a0 = {0.f, 0.f, 0.f, 0.f}, a1 = a0;
#pragma unroll
        for (int ww = 0; ww < 4; ++ww) {
            a0 += sR[(ww * 16 + (2 * p) * 4 + mf) * 64 + l];
            a1 += sR[(ww * 16 + (2 * p + 1) * 4 + mf) * 64 + l];
        }
        const int n0 = nt * 64 + p * 32 + g * 8;
        const u32x4 xv = *(const u32x4*)(x + (size_t)tok * D_ + n0);
        const f32x4 g0 = *(const f32x4*)(MOD + 1024 + n0), g1 = *(const f32x4*)(MOD + 1024 + n0 + 4);
        u32x4 o;
        o[0] = pk_bf16(bf_lo(xv[0]) + g0[0] * a0[0], bf_hi(xv[0]) + g0[1] * a0[1]);
        o[1] = pk_bf16(bf_lo(xv[1]) + g0[2] * a0[2], bf_hi(xv[1]) + g0[3] * a0[3]);
        o[2] = pk_bf16(bf_lo(xv[2]) + g1[0] * a1[0], bf_hi(xv[2]) + g1[1] * a1[1]);
        o[3] = pk_bf16(bf_lo(xv[3]) + g1[2] * a1[2], bf_hi(xv[3]) + g1[3] * a1[3]);
        X1[((size_t)(mt * 4 + mf) * 16 + (nt * 2 + p)) * 64 + l] = o;
#pragma unroll
        for (int i = 0; i < 4; ++i) { vals[p * 8 + 2 * i] = bf_lo(o[i]); vals[p * 8 + 2 * i + 1] = bf_hi(o[i]); }
    }
    float s = 0.f;
#pragma unroll
    for (int i = 0; i < 16; ++i) s += vals[i];
    s += __shfl_xor(s, 16); s += __shfl_xor(s, 32);
    const float m = s * (1.f / 64.f);
    float q = 0.f;
#pragma unroll
    for (int i = 0; i < 16; ++i) { const float d = vals[i] - m; q += d * d; }
    q += __shfl_xor(q, 16); q += __shfl_xor(q, 32);
    if (g == 0) ((f32x2*)(ws + OFF_ST))[(size_t)tok * 8 + nt] = (f32x2){m, q};
}

constexpr int K3_MT = 22, K3_NG = 11;
DI float gelu_tanh(float g) {
    const float k0 = -2.f * 0.7978845608028654f * LOG2E, k1 = k0 * 0.044715f;
    const float a = g * (g * g * k1 + k0);
    return g * frcp(1.f + fexp2(a));
}
__global__ __launch_bounds__(512) void k3_gateup(char* __restrict__ ws) {
    __shared__ __attribute__((aligned(16))) u32x4 sA[6 * 16 * 64];
    const int mt = blockIdx.x / K3_NG, ng = blockIdx.x % K3_NG;
    const int w = threadIdx.x >> 6, l = threadIdx.x & 63, g = l >> 4;
    const int mf0 = mt * 6;
    const int nmf = (NMF - mf0) < 6 ? (NMF - mf0) : 6;
    const int pair = ng * 8 + w;
    const bool active = pair < NPAIR;
    const u32x4* X1 = (const u32x4*)(ws + OFF_X1);
    const u32x4* WGU = (const u32x4*)(ws + OFF_WGU);
    const float* MOD = (const float*)(ws + OFF_MOD);
    const f32x2* ST = (const f32x2*)(ws + OFF_ST);

    {
        float mean[6], rstd[6];
#pragma unroll
        for (int mf = 0; mf < 6; ++mf) {
            const int mfc = mf < nmf ? mf : 0;
            const int tok = (mf0 + mfc) * 16 + (l & 15);
            const f32x4* sp = (const f32x4*)(ST + (size_t)tok * 8);
            const f32x4 a = sp[0], b = sp[1], c = sp[2], d = sp[3];
            const float m = ((a[0] + a[2]) + (b[0] + b[2]) + (c[0] + c[2]) + (d[0] + d[2])) * 0.125f;
            float q = (a[1] + a[3]) + (b[1] + b[3]) + (c[1] + c[3]) + (d[1] + d[3]);
            float e = 0.f;
            e += (a[0] - m) * (a[0] - m); e += (a[2] - m) * (a[2] - m); e += (b[0] - m) * (b[0] - m); e += (b[2] - m) * (b[2] - m);
            e += (c[0] - m) * (c[0] - m); e += (c[2] - m) * (c[2] - m); e += (d[0] - m) * (d[0] - m); e += (d[2] - m) * (d[2] - m);
            q += 64.f * e;
            mean[mf] = m; rstd[mf] = rsqrtf(q * (1.f / D_) + 1e-5f);
        }
#pragma unroll
        for (int kk = 0; kk < 2; ++kk) {
            const int ks = 2 * w + kk;
            const int k0 = ks * 32 + 8 * g;
            const f32x4 s0 = *(const f32x4*)(MOD + 1536 + k0), s1 = *(const f32x4*)(MOD + 1536 + k0 + 4);
            const f32x4 c0 = *(const f32x4*)(MOD + 2048 + k0), c1 = *(const f32x4*)(MOD + 2048 + k0 + 4);
            float gg[8], ss[8];
#pragma unroll
            for (int j = 0; j < 4; ++j) { gg[j] = bf_round(1.f + c0[j]); gg[4 + j] = bf_round(1.f + c1[j]); ss[j] = s0[j]; ss[4 + j] = s1[j]; }
#pragma unroll
            for (int mf = 0; mf < 6; ++mf) {
                const int mfc = mf < nmf ? mf : 0;
                const u32x4 xv = X1[((size_t)(mf0 + mfc) * 16 + ks) * 64 + l];
                u32x4 o;
#pragma unroll
                for (int j = 0; j < 4; ++j) {
                    const float t0 = (bf_lo(xv[j]) - mean[mf]) * rstd[mf], t1 = (bf_hi(xv[j]) - mean[mf]) * rstd[mf];
                    o[j] = pk_bf16(t0 * gg[2 * j] + ss[2 * j], t1 * gg[2 * j + 1] + ss[2 * j + 1]);
                }
                sA[(mf * 16 + ks) * 64 + l] = o;
            }
        }
    }
    const int pc = active ? pair : 0;
    u32x4 bg[16], bu[16];
#pragma unroll
    for (int ks = 0; ks < 16; ++ks) {
        bg[ks] = WGU[((size_t)(pc * 2) * 16 + ks) * 64 + l];
        bu[ks] = WGU[((size_t)(pc * 2 + 1) * 16 + ks) * 64 + l];
    }
    __syncthreads();
    if (!active) return;
    f32x4 ag[6], au[6];
#pragma unroll
    for (int mf = 0; mf < 6; ++mf) { ag[mf] = (f32x4){0.f, 0.f, 0.f, 0.f}; au[mf] = ag[mf]; }
#pragma unroll
    for (int ks = 0; ks < 16; ++ks) {
#pragma unroll
        for (int mf = 0; mf < 6; ++mf) {
            const u32x4 a = sA[(mf * 16 + ks) * 64 + l];
            ag[mf] = MFMA32(bg[ks], a, ag[mf]);
            au[mf] = MFMA32(bu[ks], a, au[mf]);
        }
    }
    char* HH = ws + OFF_HH;
#pragma unroll
    for (int mf = 0; mf < 6; ++mf) {
        if (mf < nmf) {
            u32x2 o;
            o[0] = pk_bf16(gelu_tanh(ag[mf][0]) * au[mf][0], gelu_tanh(ag[mf][1]) * au[mf][1]);
            o[1] = pk_bf16(gelu_tanh(ag[mf][2]) * au[mf][2], gelu_tanh(ag[mf][3]) * au[mf][3]);
            *(u32x2*)(HH + ((size_t)(mf0 + mf) * KS_F + (pair >> 1)) * 1024 + l * 16 + (pair & 1) * 8) = o;
        }
    }
}

__global__ __launch_bounds__(256) void k4_down(char* __restrict__ ws, u16* __restrict__ out) {
    __shared__ __attribute__((aligned(16))) f32x4 sR[4 * 16 * 64];
    const int mt = blockIdx.x >> 3, nt = blockIdx.x & 7;
    const int w = threadIdx.x >> 6, l = threadIdx.x & 63, g = l >> 4;
    const u32x4* HH = (const u32x4*)(ws + OFF_HH);
    const u32x4* WD = (const u32x4*)(ws + OFF_WD);
    const float* MOD = (const float*)(ws + OFF_MOD);
    f32x4 acc[4][4];
#pragma unroll
    for (int a = 0; a < 4; ++a)
#pragma unroll
        for (int b = 0; b < 4; ++b) acc[a][b] = (f32x4){0.f, 0.f, 0.f, 0.f};
#pragma unroll
    for (int i = 0; i < 11; ++i) {
        const int ks = w + 4 * i;
        if (ks < KS_F) {
            u32x4 wf[4], hf[4];
#pragma unroll
            for (int nf = 0; nf < 4; ++nf) wf[nf] = WD[((size_t)(nt * 4 + nf) * KS_F + ks) * 64 + l];
#pragma unroll
            for (int mf = 0; mf < 4; ++mf) hf[mf] = HH[((size_t)(mt * 4 + mf) * KS_F + ks) * 64 + l];
#pragma unroll
            for (int nf = 0; nf < 4; ++nf)
#pragma unroll
                for (int mf = 0; mf < 4; ++mf) acc[nf][mf] = MFMA32(wf[nf], hf[mf], acc[nf][mf]);
        }
    }
#pragma unroll
    for (int nf = 0; nf < 4; ++nf)
#pragma unroll
        for (int mf = 0; mf < 4; ++mf) sR[(w * 16 + nf * 4 + mf) * 64 + l] = acc[nf][mf];
    __syncthreads();
    const int mf = w;
    const int tok = (mt * 4 + mf) * 16 + (l & 15);
    const u32x4* X1 = (const u32x4*)(ws + OFF_X1);
#pragma unroll
    for (int p = 0; p < 2; ++p) {
        f32x4 a0 = {0.f, 0.f, 0.f, 0.f}, a1 = a0;
#pragma unroll
        for (int ww = 0; ww < 4; ++ww) {
            a0 += sR[(ww * 16 + (2 * p) * 4 + mf) * 64 + l];
            a1 += sR[(ww * 16 + (2 * p + 1) * 4 + mf) * 64 + l];
        }
        const int n0 = nt * 64 + p * 32 + g * 8;
        const u32x4 xv = X1[((size_t)(mt * 4 + mf) * 16 + (nt * 2 + p)) * 64 + l];
        const f32x4 g0 = *(const f32x4*)(MOD + 2560 + n0), g1 = *(const f32x4*)(MOD + 2560 + n0 + 4);
        u32x4 o;
        o[0] = pk_bf16(bf_lo(xv[0]) + g0[0] * a0[0], bf_hi(xv[0]) + g0[1] * a0[1]);
        o[1] = pk_bf16(bf_lo(xv[1]) + g0[2] * a0[2], bf_hi(xv[1]) + g0[3] * a0[3]);
        o[2] = pk_bf16(bf_lo(xv[2]) + g1[0] * a1[0], bf_hi(xv[2]) + g1[1] * a1[1]);
        o[3] = pk_bf16(bf_lo(xv[3]) + g1[2] * a1[2], bf_hi(xv[3]) + g1[3] * a1[3]);
        *(u32x4*)(out + (size_t)tok * D_ + n0) = o;
    }
}

extern "C" void kernel_launch(void* const* d_in, const int* in_sizes, int n_in,
                              void* d_out, int out_size, void* d_ws, size_t ws_size,
                              hipStream_t stream) {
    const u16* x = (const u16*)d_in[0];
    const u16* wqkv = (const u16*)d_in[1];
    const u16* wphi = (const u16*)d_in[2];
    const u16* wo = (const u16*)d_in[3];
    const u16* wg = (const u16*)d_in[5];
    const u16* wu = (const u16*)d_in[6];
    const u16* wd = (const u16*)d_in[7];
    const u16* cvec = (const u16*)d_in[8];
    const u16* wmod = (const u16*)d_in[9];
    char* ws = (char*)d_ws;
    k0_prep<<<K0_GRID, 256, 0, stream>>>(x, wqkv, wphi, wo, wg, wu, wd, cvec, wmod, ws);
    k1_qkv<<<256, 256, 0, stream>>>(ws);
    k2_wo<<<256, 256, 0, stream>>>(x, ws);
    k3_gateup<<<K3_MT * K3_NG, 512, 0, stream>>>(ws);
    k4_down<<<256, 256, 0, stream>>>(ws, (u16*)d_out);
}
```

```cpp
#include <hip/hip_runtime.h>
#include <hip/hip_bf16.h>
#include <stdint.h>

typedef short bf16x8 __attribute__((ext_vector_type(8)));
typedef short bf16x4 __attribute__((ext_vector_type(4)));
typedef float f32x4 __attribute__((ext_vector_type(4)));
typedef float f32x2 __attribute__((ext_vector_type(2)));
typedef unsigned u32x4 __attribute__((ext_vector_type(4)));
typedef unsigned u32x2 __attribute__((ext_vector_type(2)));
typedef __bf16 bf16x2_t __attribute__((ext_vector_type(2)));
typedef unsigned short u16;

#define PT_ST 2
#define K3_LA 4
#define DI __device__ __forceinline__
#define MFMA32(a, b, c) __builtin_amdgcn_mfma_f32_16x16x32_bf16(__builtin_bit_cast(bf16x8, (a)), __builtin_bit_cast(bf16x8, (b)), (c), 0, 0, 0)
#define SB() __builtin_amdgcn_sched_barrier(0)

constexpr int S_ = 2048, D_ = 512, H_ = 32, F_ = 1376;
constexpr int NMF = S_ / 16;
constexpr int KS_D = D_ / 32;
constexpr int KS_F = F_ / 32;
constexpr int NPAIR = F_ / 16;
constexpr float LOG2E = 1.4426950408889634f;

constexpr size_t OFF_MOD  = 0;
constexpr size_t OFF_CNT  = 8192;
constexpr size_t OFF_KVF  = OFF_CNT + 49152;
constexpr size_t OFF_ZF   = OFF_KVF + 32768;
constexpr size_t OFF_SLAB = OFF_ZF + 4096;
constexpr size_t OFF_ZSL  = OFF_SLAB + (size_t)32 * 32 * 1024;
constexpr size_t OFF_XN   = OFF_ZSL + (size_t)32 * 32 * 128;
constexpr size_t OFF_WQKV = OFF_XN + (size_t)S_ * D_ * 2;
constexpr size_t OFF_WO   = OFF_WQKV + (size_t)96 * 16384;
constexpr size_t OFF_WGU  = OFF_WO + (size_t)32 * 16384;
constexpr size_t OFF_WD   = OFF_WGU + (size_t)88 * 2 * 16384;
constexpr size_t OFF_PHIQ = OFF_WD + (size_t)32 * KS_F * 1024;
constexpr size_t OFF_X1   = OFF_PHIQ + (size_t)NMF * 32 * 1024;
constexpr size_t OFF_ST   = OFF_X1 + (size_t)S_ * D_ * 2;
constexpr size_t OFF_C12  = OFF_ST + (size_t)S_ * 16 * 8;
constexpr size_t OFF_WGU2 = OFF_C12 + (size_t)176 * 128;
constexpr size_t OFF_HH   = OFF_WGU2 + (size_t)88 * 2 * 16384;

#define TS_DECL
#define TS(idx)
#define TS_FLUSH(kid)
#define TSW(idx)
#define TSW_FLUSH(kid)

DI unsigned pk_bf16(float a, float b) {
    f32x2 v = {a, b};
    bf16x2_t r = __builtin_convertvector(v, bf16x2_t);
    return __builtin_bit_cast(unsigned, r);
}
DI float bf_lo(unsigned u) { return __uint_as_float(u << 16); }
DI float bf_hi(unsigned u) { return __uint_as_float(u & 0xffff0000u); }
DI float bf_round(float a) { return bf_lo(pk_bf16(a, 0.f)); }
DI float fexp2(float x) { return __builtin_amdgcn_exp2f(x); }
DI float frcp(float x) { return __builtin_amdgcn_rcpf(x); }

template <int KMAP, int NMAP>
DI void pretile_wave(const u16* __restrict__ W, int ldw, int ncols, int ks, int cgrp, u32x4* __restrict__ dst, int nks, int nf_mul, int nf_add,
                     u32x4* sm  ) {
    const int l = threadIdx.x & 63, g = l >> 4, c8 = l & 15;
    const int col0 = cgrp * 128 + c8 * 8;
    const bool ok = col0 < ncols;
    const int colc = ok ? col0 : 0;
    u32x4 r[8];
#pragma unroll
    for (int j = 0; j < 8; ++j) {
        const int k = ks * 32 + (KMAP == 0 ? 8 * g + j : 16 * (j >> 2) + 4 * g + (j & 3));
        r[j] = *(const u32x4*)(W + (size_t)k * ldw + colc);
    }
#pragma unroll
    for (int c = 0; c < 8; ++c) {
        u32x4 o;
#pragma unroll
        for (int jj = 0; jj < 4; ++jj) {
            const unsigned a = r[2 * jj][c >> 1], b = r[2 * jj + 1][c >> 1];
            o[jj] = (c & 1) ? ((a >> 16) | (b & 0xffff0000u)) : ((a & 0xffffu) | (b << 16));
        }
        const int n = c8 * 8 + c;
        int nf, rr;
        if (NMAP == 0) { nf = n >> 4; rr = n & 15; }
        else { const int cc = n & 31; nf = 2 * (n >> 5) + ((cc >> 2) & 1); rr = 4 * (cc >> 3) + (cc & 3); }
        sm[nf * 65 + g * 16 + rr] = o;
    }
    asm volatile("s_waitcnt lgkmcnt(0)" ::: "memory");
    __builtin_amdgcn_wave_barrier();
#pragma unroll
    for (int nf = 0; nf < 8; ++nf) {
        const int nfg = cgrp * 8 + nf;
        if (nfg * 16 < ncols) {
            const u32x4 o = sm[nf * 65 + l];
            u32x4* p_ = &dst[((size_t)(nfg * nf_mul + nf_add) * nks + ks) * 64 + l];
            asm volatile("global_store_dwordx4 %0, %1, off sc1\n\ts_nop 1" :: "v"(p_), "v"(o) : "memory");
        }
    }
}

constexpr int K0_MOD = 96, K0_XN = 128, K0_FOLD = 64, K0_WV = 16, K0_WO = 16, K0_WG = 44, K0_WU = 44, K0_WD = 43;
constexpr int K0_B0 = K0_MOD, K0_B1 = K0_B0 + K0_XN, K0_B2 = K0_B1 + K0_FOLD, K0_B3 = K0_B2 + K0_WV;
constexpr int K0_B4 = K0_B3 + K0_WO, K0_B5 = K0_B4 + K0_WG, K0_B6 = K0_B5 + K0_WU, K0_B7 = K0_B6 + K0_WD;
constexpr int K0_GRID = K0_B7;

__global__ __launch_bounds__(256) void k0_prep(const u16* __restrict__ x, const u16* __restrict__ wqkv, const u16* __restrict__ wphi,
                                               const u16* __restrict__ cvec, const u16* __restrict__ wmod,
                                               const u16* __restrict__ wo, const u16* __restrict__ wg, const u16* __restrict__ wu,
                                               const u16* __restrict__ wd, char* __restrict__ ws) {
    __shared__ __attribute__((aligned(16))) u32x4 smT[4 * 520];
    float* red = (float*)smT;
    TS_DECL; TS(0);
    const int b = blockIdx.x, t = threadIdx.x;
    if (b == K0_B0 && t < 192) ((u32x4*)(ws + OFF_CNT))[t * 16] = (u32x4){0u, 0u, 0u, 0u};
    if (b < K0_B0) {
        const int cb = (b & 7) * 12 + (b >> 3);
        const int cgp = t & 3, kg = t >> 2;
        float acc[8];
#pragma unroll
        for (int i = 0; i < 8; ++i) acc[i] = 0.f;
        u32x4 wv[8]; float sc[8];
#pragma unroll
        for (int i = 0; i < 8; ++i) {
            const int k = kg + 64 * i;
            wv[i] = *(const u32x4*)(wmod + (size_t)k * 3072 + cb * 32 + cgp * 8);
            const float c = bf_lo(cvec[k]);
            sc[i] = bf_round(c / (1.f + expf(-c)));
        }
#pragma unroll
        for (int i = 0; i < 8; ++i) {
#pragma unroll
            for (int j = 0; j < 4; ++j) {
                acc[2 * j] += sc[i] * bf_lo(wv[i][j]);
                acc[2 * j + 1] += sc[i] * bf_hi(wv[i][j]);
            }
        }
#pragma unroll
        for (int j = 0; j < 8; ++j) {
            acc[j] += __shfl_xor(acc[j], 4); acc[j] += __shfl_xor(acc[j], 8); acc[j] += __shfl_xor(acc[j], 16); acc[j] += __shfl_xor(acc[j], 32);
        }
        if ((t & 63) < 4) {
#pragma unroll
            for (int j = 0; j < 8; ++j) red[(t >> 6) * 32 + cgp * 8 + j] = acc[j];
        }
        __syncthreads();
        if (t < 32) {
            float s = bf_round((red[t] + red[32 + t]) + (red[64 + t] + red[96 + t]));
            const int n = cb * 32 + t, row = n >> 9;
            if (row == 1 || row == 4) s = 1.f + s;
            ((u16*)(ws + OFF_MOD))[n] = (u16)(pk_bf16(s, 0.f) & 0xffffu);
        }
    } else if (b < K0_B1) {
        const int mf = b - K0_B0, r = t & 15, c = t >> 4;
        const u16* xr = x + (size_t)(mf * 16 + r) * D_;
        u32x4 v[4];
#pragma unroll
        for (int i = 0; i < 4; ++i) v[i] = *(const u32x4*)(xr + (c + 16 * i) * 8);
        float f[32];
        float s = 0.f;
#pragma unroll
        for (int i = 0; i < 4; ++i)
#pragma unroll
            for (int j = 0; j < 4; ++j) { f[i * 8 + 2 * j] = bf_lo(v[i][j]); f[i * 8 + 2 * j + 1] = bf_hi(v[i][j]); s += f[i * 8 + 2 * j] + f[i * 8 + 2 * j + 1]; }
        s += __shfl_xor(s, 16); s += __shfl_xor(s, 32);
        if ((t & 63) < 16) red[(t >> 6) * 16 + r] = s;
        __syncthreads();
        const float m = ((red[r] + red[16 + r]) + (red[32 + r] + red[48 + r])) * (1.f / D_);
        float q = 0.f;
#pragma unroll
        for (int i = 0; i < 32; ++i) { f[i] -= m; q += f[i] * f[i]; }
        q += __shfl_xor(q, 16); q += __shfl_xor(q, 32);
        if ((t & 63) < 16) red[64 + (t >> 6) * 16 + r] = q;
        __syncthreads();
        const float var = (red[64 + r] + red[80 + r]) + (red[96 + r] + red[112 + r]);
        const float rs = rsqrtf(var * (1.f / D_) + 1e-5f);
        u32x4* dst = (u32x4*)(ws + OFF_XN) + (size_t)mf * 16 * 64;
#pragma unroll
        for (int i = 0; i < 4; ++i) {
            u32x4 o;
#pragma unroll
            for (int j = 0; j < 4; ++j) o[j] = pk_bf16(f[i * 8 + 2 * j] * rs, f[i * 8 + 2 * j + 1] * rs);
            const int ch = c + 16 * i;
            dst[(ch >> 2) * 64 + (ch & 3) * 16 + r] = o;
        }
    } else if (b < K0_B2) {
        const int wv = (b - K0_B1) * 4 + (t >> 6);
        const int hq = wv >> 2, kq = wv & 3;
        const int l = t & 63, r = l & 15, g = l >> 4, h = hq & 31;
        bf16x4 bphi;
#pragma unroll
        for (int j = 0; j < 4; ++j) bphi[j] = (short)wphi[h * 256 + (4 * g + j) * 16 + r];
        char* dst = ws + OFF_WQKV + (size_t)hq * 16384;
        bf16x4 a[8];
#pragma unroll
        for (int i = 0; i < 8; ++i) a[i] = *(const bf16x4*)(wqkv + (size_t)((kq * 8 + i) * 16 + r) * 1536 + hq * 16 + 4 * g);
#pragma unroll
        for (int i = 0; i < 8; ++i) {
            const int rb = kq * 8 + i;
            f32x4 d = {0.f, 0.f, 0.f, 0.f};
            d = __builtin_amdgcn_mfma_f32_16x16x16bf16_1k(a[i], bphi, d, 0, 0, 0);
            u32x2 o;
            o[0] = pk_bf16(d[0] * LOG2E, d[1] * LOG2E);
            o[1] = pk_bf16(d[2] * LOG2E, d[3] * LOG2E);
            const int ks = rb >> 1, gp = 2 * (rb & 1) + (g >> 1);
            *(u32x2*)(dst + ks * 1024 + (gp * 16 + r) * 16 + (g & 1) * 8) = o;
        }
    } else {
        u32x4* sm = smT + (t >> 6) * 520;
        if (b < K0_B3) {
            const int id = (b - K0_B2) * 4 + (t >> 6);
            pretile_wave<0, 0>(wqkv + 1024, 1536, 512, id >> 2, id & 3, (u32x4*)(ws + OFF_WQKV), KS_D, 1, 64, sm);
        } else if (b < K0_B4) {
            const int id = (b - K0_B3) * 4 + (t >> 6);
            pretile_wave<1, 1>(wo, 512, 512, id >> 2, id & 3, (u32x4*)(ws + OFF_WO), KS_D, 1, 0, sm);
        } else if (b < K0_B5) {
            const int id = (b - K0_B4) * 4 + (t >> 6);
            pretile_wave<0, 0>(wg, F_, F_, id / 11, id % 11, (u32x4*)(ws + OFF_WGU), KS_D, 2, 0, sm);
        } else if (b < K0_B6) {
            const int id = (b - K0_B5) * 4 + (t >> 6);
            pretile_wave<0, 0>(wu, F_, F_, id / 11, id % 11, (u32x4*)(ws + OFF_WGU), KS_D, 2, 1, sm);
        } else {
            const int id = (b - K0_B6) * 4 + (t >> 6);
            pretile_wave<1, 1>(wd, 512, 512, id >> 2, id & 3, (u32x4*)(ws + OFF_WD), KS_F, 1, 0, sm);
        }
    }
    TS_FLUSH(0);
}

DI float u2f(unsigned u) { return __uint_as_float(u); }
DI unsigned f2u(float f) { return __float_as_uint(f); }
DI void red2_max(float a, float b, float& A, float& B) {
    auto r = __builtin_amdgcn_permlane32_swap(f2u(a), f2u(b), false, false);
    const float c = fmaxf(u2f(r[0]), u2f(r[1]));
    auto r2 = __builtin_amdgcn_permlane16_swap(f2u(c), f2u(c), false, false);
    const float d = fmaxf(u2f(r2[0]), u2f(r2[1]));
    auto r3 = __builtin_amdgcn_permlane32_swap(f2u(d), f2u(d), false, false);
    A = u2f(r3[0]); B = u2f(r3[1]);
}
DI void red2_add(float a, float b, float& A, float& B) {
    auto r = __builtin_amdgcn_permlane32_swap(f2u(a), f2u(b), false, false);
    const float c = u2f(r[0]) + u2f(r[1]);
    auto r2 = __builtin_amdgcn_permlane16_swap(f2u(c), f2u(c), false, false);
    const float d = u2f(r2[0]) + u2f(r2[1]);
    auto r3 = __builtin_amdgcn_permlane32_swap(f2u(d), f2u(d), false, false);
    A = u2f(r3[0]); B = u2f(r3[1]);
}
DI float fmax2(float a, float b) { return __builtin_amdgcn_fmed3f(a, b, __builtin_inff()); }
DI float fmin2(float a, float b) { return __builtin_amdgcn_fmed3f(a, b, -__builtin_inff()); }
template <int N>
DI void softmax_pm(const f32x4 (&p)[N], u32x4 (&o)[N]) {
    float a[N], b[N];
#pragma unroll
    for (int i = 0; i < N; ++i) {
        a[i] = fmax2(fmax2(p[i][0], p[i][1]), fmax2(p[i][2], p[i][3]));
        b[i] = -fmin2(fmin2(p[i][0], p[i][1]), fmin2(p[i][2], p[i][3]));
    }
#pragma unroll
    for (int i = 0; i < N; ++i) { auto r = __builtin_amdgcn_permlane32_swap(f2u(a[i]), f2u(b[i]), false, false); a[i] = fmax2(u2f(r[0]), u2f(r[1])); }
#pragma unroll
    for (int i = 0; i < N; ++i) { auto r = __builtin_amdgcn_permlane16_swap(f2u(a[i]), f2u(a[i]), false, false); a[i] = fmax2(u2f(r[0]), u2f(r[1])); }
#pragma unroll
    for (int i = 0; i < N; ++i) { auto r = __builtin_amdgcn_permlane32_swap(f2u(a[i]), f2u(a[i]), false, false); a[i] = u2f(r[0]); b[i] = u2f(r[1]); }
    float ep[N][4], en[N][4];
#pragma unroll
    for (int i = 0; i < N; ++i)
#pragma unroll
        for (int j = 0; j < 4; ++j) { ep[i][j] = fexp2(p[i][j] - a[i]); en[i][j] = fexp2(-b[i] - p[i][j]); }
#pragma unroll
    for (int i = 0; i < N; ++i) { a[i] = (ep[i][0] + ep[i][1]) + (ep[i][2] + ep[i][3]); b[i] = (en[i][0] + en[i][1]) + (en[i][2] + en[i][3]); }
#pragma unroll
    for (int i = 0; i < N; ++i) { auto r = __builtin_amdgcn_permlane32_swap(f2u(a[i]), f2u(b[i]), false, false); a[i] = u2f(r[0]) + u2f(r[1]); }
#pragma unroll
    for (int i = 0; i < N; ++i) { auto r = __builtin_amdgcn_permlane16_swap(f2u(a[i]), f2u(a[i]), false, false); a[i] = frcp(u2f(r[0]) + u2f(r[1])); }
#pragma unroll
    for (int i = 0; i < N; ++i) { auto r = __builtin_amdgcn_permlane32_swap(f2u(a[i]), f2u(a[i]), false, false); a[i] = u2f(r[0]); b[i] = u2f(r[1]); }
#pragma unroll
    for (int i = 0; i < N; ++i) {
        o[i][0] = pk_bf16(ep[i][0] * a[i], ep[i][1] * a[i]); o[i][1] = pk_bf16(ep[i][2] * a[i], ep[i][3] * a[i]);
        o[i][2] = pk_bf16(en[i][0] * b[i], en[i][1] * b[i]); o[i][3] = pk_bf16(en[i][2] * b[i], en[i][3] * b[i]);
    }
}

DI u32x4 modulate8(const u32x4 xv, const float (&gg)[8], const float (&ss)[8]) {
    u32x4 o;
#pragma unroll
    for (int j = 0; j < 4; ++j)
        o[j] = pk_bf16(bf_lo(xv[j]) * gg[2 * j] + ss[2 * j], bf_hi(xv[j]) * gg[2 * j + 1] + ss[2 * j + 1]);
    return o;
}

#define TSA(i) TS(i)
#define TSB(i)
#define TSC(i)
#define RLX_AGENT __ATOMIC_RELAXED, __HIP_MEMORY_SCOPE_AGENT
constexpr int CNT_CK = 0, CNT_CV = 32, CNT_CP = 64, CNT_CX = 96, CNT_CH = 128, CNT_CW = 150, CNT_CT = 170;
typedef __amdgpu_buffer_rsrc_t rsrc_t;
DI u32x4 bld(rsrc_t rs, unsigned off) { return __builtin_amdgcn_raw_buffer_load_b128(rs, off, 0, 16); }
DI u32x2 bld64(rsrc_t rs, unsigned off) { return __builtin_amdgcn_raw_buffer_load_b64(rs, off, 0, 16); }
DI void bst(rsrc_t rs, unsigned off, u32x4 v) { __builtin_amdgcn_raw_buffer_store_b128(v, rs, off, 0, 16); }
DI void bst64(rsrc_t rs, unsigned off, u32x2 v) { __builtin_amdgcn_raw_buffer_store_b64(v, rs, off, 0, 16); }
#define POLL_SLEEP 4
#define DRAIN() asm volatile("s_waitcnt vmcnt(0)" ::: "memory")
DI void wait_cnt(unsigned* c, unsigned target) {
    unsigned sp = 0;
    while (__hip_atomic_load(c, RLX_AGENT) < target) { __builtin_amdgcn_s_sleep(POLL_SLEEP); if (++sp > (1u << 18)) break; }
}
DI float gelu_tanh(float g) {
    const float k0 = -2.f * 0.7978845608028654f * LOG2E, k1 = k0 * 0.044715f;
    const float a = g * (g * g * k1 + k0);
    return g * frcp(1.f + fexp2(a));
}
DI u32x4 scale8(const u32x4 v, const u32x4 gv) {
    u32x4 o;
#pragma unroll
    for (int j = 0; j < 4; ++j) o[j] = pk_bf16(bf_lo(v[j]) * bf_lo(gv[j]), bf_hi(v[j]) * bf_hi(gv[j]));
    return o;
}

#define B3_EARLY 10
#define B3_MID 12
extern "C" __global__ __launch_bounds__(512, 2) void kf_main(const u16* __restrict__ x, char* __restrict__ ws, u16* __restrict__ out) {
    __shared__ __attribute__((aligned(16))) u32x4 smem[9216 + 128 + 128 + 64 + 128 + 4];
    TS_DECL; TS(0);
    const int xcd = blockIdx.x & 7, bi = blockIdx.x >> 3;
    const int wv = __builtin_amdgcn_readfirstlane(threadIdx.x >> 6);
    const int l = threadIdx.x & 63, g = l >> 4;
    unsigned* cnt = (unsigned*)(ws + OFF_CNT);
    const rsrc_t rs = __builtin_amdgcn_make_buffer_rsrc(ws, 0, 0x4000000, 0x00020000);
    u32x4* sWO = smem + 5120;
    u32x4* sZ = smem + 9216;
    u32x4* sGB = smem + 9216 + 128;
    f32x2* sSt = (f32x2*)(smem + 9216 + 256);
    volatile unsigned* sFlag = (volatile unsigned*)(smem + 9216 + 448);
    const u32x4 ones = {0x3f803f80u, 0x3f803f80u, 0x3f803f80u, 0x3f803f80u};
    const f32x4 zero = {0.f, 0.f, 0.f, 0.f};

    const int mt2 = 4 * xcd + (bi >> 3), nt2 = bi & 7;
    const int kq = wv & 3, mh = wv >> 2;
    const int t0_ = (242 * xcd) >> 3, t1_ = (242 * (xcd + 1)) >> 3;
    const int id3 = t0_ + bi;
    const bool act3 = id3 < t1_;
    const int id3c = act3 ? id3 : t0_;
    const int ng = (id3c % 121) / 11, mt3 = 11 * (id3c / 121) + (id3c % 121) % 11;
    const int mf0 = mt3 * 6;
    const int nmf = (NMF - mf0) < 6 ? (NMF - mf0) : 6;
    const int pair = ng * 8 + wv;
    const bool actw = pair < NPAIR;
    const unsigned wgoff = (unsigned)(OFF_WGU2 + ((size_t)(actw ? pair : ng * 8) * 2048 + l) * 16);
    u32x4 bg[16], bu[16];
#define K3_LOADB(ks) do { bg[ks] = bld(rs, wgoff + (ks) * 1024); bu[ks] = bld(rs, wgoff + 16384 + (ks) * 1024); SB(); } while (0)

#define WO_DMA(part) do { const u32x4* WO_ = (const u32x4*)(ws + OFF_WO) + ((size_t)nt2 * 64 + (part) * 8) * 64 + l; \
        const unsigned ldsb_ = (unsigned)(size_t)(sWO + (part) * 8 * 64); \
        _Pragma("unroll") for (int i_ = 0; i_ < 8; ++i_) { unsigned keep_; \
            asm volatile("s_mov_b32 %0, m0\n\ts_mov_b32 m0, %2\n\ts_nop 0\n\tglobal_load_lds_dwordx4 %1, off\n\ts_mov_b32 m0, %0" : "=&s"(keep_) : "v"(WO_ + i_ * 64), "s"(ldsb_ + i_ * 1024) : "memory"); } } while (0)
    if (threadIdx.x == 0) { sFlag[0] = 0u; sFlag[1] = 0u; sFlag[2] = 0u; sFlag[3] = 0u; }
    {
        u32x4* sA = smem;
        char* sP = (char*)(smem + 4096);
        const int mt = 8 * (xcd >> 1) + (bi >> 2), hg = 4 * (xcd & 1) + (bi & 3);
        const int w = wv & 3;
        const int h = hg * 4 + w;
        const u32x4* WQ = (const u32x4*)(ws + OFF_WQKV) + (size_t)h * 1024 + l;
        u32x4 af[2][4];
#define K1_RDA(buf, ks) do { _Pragma("unroll") for (int mf = 0; mf < 4; ++mf) af[buf][mf] = sA[(mf * 16 + (ks)) * 64 + l]; } while (0)
        if (wv < 4) {
            __builtin_amdgcn_s_setprio(3);
            const u32x4* WK = WQ + 32 * 1024;
            const u32x4* WV = WQ + 64 * 1024;
            u32x4 bk[16], bv[16];
#define K1_LOADKV(ks) do { bk[ks] = WK[(ks) * 64]; bv[ks] = WV[(ks) * 64]; SB(); } while (0)
            f32x4 ak[4], av[4];
#pragma unroll
            for (int mf = 0; mf < 4; ++mf) { ak[mf] = zero; av[mf] = zero; }
#pragma unroll
            for (int i = 0; i < 6; ++i) K1_LOADKV(i);
            __syncthreads(); SB();
            K1_RDA(0, 0);
#pragma unroll
            for (int ks = 0; ks < 16; ++ks) {
                if (ks + 6 < 16) K1_LOADKV(ks + 6);
                if (ks == 3 || ks == 7 || ks == 11) { __syncthreads(); SB(); }
                if (ks + 1 < 16) K1_RDA((ks + 1) & 1, ks + 1);
                SB();
#pragma unroll
                for (int mf = 0; mf < 4; ++mf) { ak[mf] = MFMA32(bk[ks], af[ks & 1][mf], ak[mf]); av[mf] = MFMA32(af[ks & 1][mf], bv[ks], av[mf]); }
                SB();
            }
            TSC(1);
            char* myP = sP + w * 4096;
            {
                u32x4 o[4];
                softmax_pm<4>(ak, o);
#pragma unroll
                for (int mf = 0; mf < 4; ++mf) {
                    char* row = myP + (mf * 16 + (l & 15)) * 64;
                    *(u32x2*)(row + g * 8) = (u32x2){o[mf][0], o[mf][1]};
                    *(u32x2*)(row + 32 + g * 8) = (u32x2){o[mf][2], o[mf][3]};
                }
            }
            TSC(2);
            const unsigned base = (unsigned)(size_t)myP + (4 * g + ((l & 15) >> 2)) * 64 + (l & 3) * 8;
            u32x2 t0, t1, t2, t3, t4, t5, t6, t7;
            asm volatile("s_waitcnt lgkmcnt(0)\n\t"
                         "ds_read_b64_tr_b16 %0, %8\n\tds_read_b64_tr_b16 %1, %8 offset:1024\n\t"
                         "ds_read_b64_tr_b16 %2, %8 offset:32\n\tds_read_b64_tr_b16 %3, %8 offset:1056\n\t"
                         "ds_read_b64_tr_b16 %4, %8 offset:2048\n\tds_read_b64_tr_b16 %5, %8 offset:3072\n\t"
                         "ds_read_b64_tr_b16 %6, %8 offset:2080\n\tds_read_b64_tr_b16 %7, %8 offset:3104\n\t"
                         "s_waitcnt lgkmcnt(0)"
                         : "=&v"(t0), "=&v"(t1), "=&v"(t2), "=&v"(t3), "=&v"(t4), "=&v"(t5), "=&v"(t6), "=&v"(t7) : "v"(base) : "memory");
            if (l == 0) __hip_atomic_fetch_add((unsigned*)&sFlag[2], 16u, __ATOMIC_RELAXED, __HIP_MEMORY_SCOPE_WORKGROUP);
            const u32x4 pa00 = {t0[0], t0[1], t1[0], t1[1]}, pa01 = {t2[0], t2[1], t3[0], t3[1]};
            const u32x4 pa10 = {t4[0], t4[1], t5[0], t5[1]}, pa11 = {t6[0], t6[1], t7[0], t7[1]};
            u32x4 vb0, vb1;
            vb0[0] = pk_bf16(av[0][0], av[0][1]); vb0[1] = pk_bf16(av[0][2], av[0][3]); vb0[2] = pk_bf16(av[1][0], av[1][1]); vb0[3] = pk_bf16(av[1][2], av[1][3]);
            vb1[0] = pk_bf16(av[2][0], av[2][1]); vb1[1] = pk_bf16(av[2][2], av[2][3]); vb1[2] = pk_bf16(av[3][0], av[3][1]); vb1[3] = pk_bf16(av[3][2], av[3][3]);
            f32x4 kv0 = MFMA32(pa00, vb0, zero), kv1 = MFMA32(pa01, vb0, zero);
            f32x4 z0 = MFMA32(pa00, ones, zero), z1 = MFMA32(pa01, ones, zero);
            kv0 = MFMA32(pa10, vb1, kv0); kv1 = MFMA32(pa11, vb1, kv1);
            z0 = MFMA32(pa10, ones, z0); z1 = MFMA32(pa11, ones, z1);
            u32x4 o;
            o[0] = pk_bf16(kv0[0], kv0[1]); o[1] = pk_bf16(kv0[2], kv0[3]); o[2] = pk_bf16(kv1[0], kv1[1]); o[3] = pk_bf16(kv1[2], kv1[3]);
            bst(rs, (unsigned)(OFF_SLAB + (((size_t)h * 32 + mt) * 64 + l) * 16), o);
            if ((l & 15) < 2) {
                const f32x4 zz = (l & 1) ? z1 : z0;
                bst(rs, (unsigned)(OFF_ZSL + (((size_t)h * 32 + mt) * 8 + g * 2 + (l & 1)) * 16), __builtin_bit_cast(u32x4, zz));
            }
            TSC(3);
            DRAIN();
            TSC(4);
            if (l == 0) __hip_atomic_fetch_add(cnt + (CNT_CK + h) * 64, 1u, RLX_AGENT);
            TSA(1);
            __builtin_amdgcn_s_setprio(0);
            if (wv == 0) {
                const int hr = blockIdx.x >> 3, sr = blockIdx.x & 7, sg = l >> 3, e = l & 7;
                wait_cnt(cnt + (CNT_CK + hr) * 64, 32u);
                u32x4 sl[4], zl[4];
        #pragma unroll
                for (int i = 0; i < 4; ++i) sl[i] = bld(rs, (unsigned)(OFF_SLAB + (((size_t)hr * 32 + 8 * i + sg) * 64 + sr * 8 + e) * 16));
                if (sr == 0) {
        #pragma unroll
                    for (int i = 0; i < 4; ++i) zl[i] = bld(rs, (unsigned)(OFF_ZSL + (((size_t)hr * 32 + 8 * i + sg) * 8 + e) * 16));
                }
                float a[8];
        #pragma unroll
                for (int j = 0; j < 4; ++j) {
                    a[2 * j] = (bf_lo(sl[0][j]) + bf_lo(sl[1][j])) + (bf_lo(sl[2][j]) + bf_lo(sl[3][j]));
                    a[2 * j + 1] = (bf_hi(sl[0][j]) + bf_hi(sl[1][j])) + (bf_hi(sl[2][j]) + bf_hi(sl[3][j]));
                }
        #pragma unroll
                for (int j = 0; j < 8; ++j) a[j] += __shfl_xor(a[j], 8);
        #pragma unroll
                for (int j = 0; j < 8; j += 2) red2_add(a[j], a[j + 1], a[j], a[j + 1]);
                if (l < 8) bst(rs, (unsigned)(OFF_KVF + (hr * 64 + sr * 8 + l) * 16), (u32x4){pk_bf16(a[0], a[1]), pk_bf16(a[2], a[3]), pk_bf16(a[4], a[5]), pk_bf16(a[6], a[7])});
                if (sr == 0) {
                    float z[4];
        #pragma unroll
                    for (int j = 0; j < 4; ++j) z[j] = (u2f(zl[0][j]) + u2f(zl[1][j])) + (u2f(zl[2][j]) + u2f(zl[3][j]));
        #pragma unroll
                    for (int j = 0; j < 4; ++j) z[j] += __shfl_xor(z[j], 8);
                    red2_add(z[0], z[1], z[0], z[1]); red2_add(z[2], z[3], z[2], z[3]);
                    if (l < 8) bst64(rs, (unsigned)(OFF_ZF + hr * 64 + l * 8), (u32x2){pk_bf16(z[0], z[1]), pk_bf16(z[2], z[3])});
                }
                DRAIN();
                unsigned last = 0;
        if (l == 0) {
            if (__hip_atomic_fetch_add(cnt + (CNT_CV + (hr >> 1)) * 64, 1u, RLX_AGENT) == 15u)
                last = __hip_atomic_fetch_add(cnt + CNT_CT * 64, 1u, RLX_AGENT) == 15u;
        }
        last = __builtin_amdgcn_readfirstlane(last);
        if (last && l < 16) __hip_atomic_store(cnt + (CNT_CV + 16 + l) * 64, 1u, RLX_AGENT);
            }

            TSA(3);
        } else {
            const u32x4* XN = (const u32x4*)(ws + OFF_XN) + (size_t)mt * 4096 + w * 64 + l;
            const u32x4* MS = (const u32x4*)(ws + OFF_MOD) + w * 4 + g;
            u32x4 xa[4][4], gs[4][2], bq[16];
#define K1_LOADA(c) do { gs[c][0] = MS[64 + (c) * 16]; gs[c][1] = MS[(c) * 16]; \
        _Pragma("unroll") for (int mf = 0; mf < 4; ++mf) xa[c][mf] = XN[mf * 1024 + (c) * 256]; SB(); } while (0)
#define K1_PREP(c) do { float gg[8], ss[8]; \
        _Pragma("unroll") for (int j = 0; j < 4; ++j) { gg[2 * j] = bf_lo(gs[c][0][j]); gg[2 * j + 1] = bf_hi(gs[c][0][j]); ss[2 * j] = bf_lo(gs[c][1][j]); ss[2 * j + 1] = bf_hi(gs[c][1][j]); } \
        _Pragma("unroll") for (int mf = 0; mf < 4; ++mf) sA[(mf * 16 + 4 * (c) + w) * 64 + l] = modulate8(xa[c][mf], gg, ss); SB(); } while (0)
#define K1_LOADQ(c) do { _Pragma("unroll") for (int i = 0; i < 4; ++i) bq[4 * (c) + i] = WQ[(4 * (c) + i) * 64]; SB(); } while (0)
            K1_LOADA(0); K1_LOADA(1);
            K1_PREP(0); K1_LOADA(2);
            __syncthreads(); SB();
            K1_PREP(1); K1_LOADA(3); __syncthreads(); SB();
            const bool wunit = blockIdx.x < 172;
            u32x4 wr[4], wgam[4], wbet[4];
            K1_PREP(2);
            if (wunit) {
#pragma unroll
                for (int i = 0; i < 4; ++i) {
                    wr[i] = ((const u32x4*)(ws + OFF_WGU))[((size_t)blockIdx.x * 16 + 4 * w + i) * 64 + l];
                    wgam[i] = ((const u32x4*)(ws + OFF_MOD))[256 + (4 * w + i) * 4 + g];
                    wbet[i] = ((const u32x4*)(ws + OFF_MOD))[192 + (4 * w + i) * 4 + g];
                }
                SB();
            }
            __syncthreads(); SB();
            K1_PREP(3); K1_LOADQ(0); __syncthreads(); SB();
            K1_LOADQ(1); K1_LOADQ(2); K1_LOADQ(3);
            TSC(1);
            f32x4* sCW = (f32x4*)(smem + 9216 + 128);
            if (wunit) {
                f32x4 c1 = zero, c2 = zero;
#pragma unroll
                for (int i = 0; i < 4; ++i) {
                    c2 = MFMA32(wr[i], wbet[i], c2);
                    const u32x4 o = scale8(wr[i], wgam[i]);
                    c1 = MFMA32(o, ones, c1);
                    bst(rs, (unsigned)(OFF_WGU2 + (((size_t)blockIdx.x * 16 + 4 * w + i) * 64 + l) * 16), o);
                }
                if ((l & 15) == 0) { sCW[w * 8 + g] = c1; sCW[w * 8 + 4 + g] = c2; }
                asm volatile("s_waitcnt lgkmcnt(0)" ::: "memory");
                unsigned old = 0;
                if (l == 0) old = __hip_atomic_fetch_add((unsigned*)&sFlag[3], 1u, __ATOMIC_RELAXED, __HIP_MEMORY_SCOPE_WORKGROUP);
                old = __builtin_amdgcn_readfirstlane(old);
                if (old == 3u && l < 8) {
                    const f32x4 a = (sCW[l] + sCW[8 + l]) + (sCW[16 + l] + sCW[24 + l]);
                    bst(rs, (unsigned)(OFF_C12 + (size_t)blockIdx.x * 128 + l * 16), __builtin_bit_cast(u32x4, a));
                }
                SB();
            }
            TSC(2);
            { unsigned sp = 0; while ((sFlag[2] >> 4) != 4u) { __builtin_amdgcn_s_sleep(1); if (++sp > (1u << 22)) break; } }
            asm volatile("" ::: "memory");
            f32x4 aq[4];
#pragma unroll
            for (int mf = 0; mf < 4; ++mf) aq[mf] = zero;
            K1_RDA(0, 0); K1_RDA(1, 1);
#pragma unroll
            for (int ks = 0; ks < 16; ++ks) {
                SB();
#pragma unroll
                for (int mf = 0; mf < 4; ++mf) aq[mf] = MFMA32(bq[ks], af[ks & 1][mf], aq[mf]);
                SB();
                if (ks + 2 < 16) K1_RDA(ks & 1, ks + 2);
                if (ks == 3) WO_DMA(2 * w);
                if (ks == 5) WO_DMA(2 * w + 1);
            }
            TSC(3);
            u32x4 o[4];
            softmax_pm<4>(aq, o);
#pragma unroll
            for (int mf = 0; mf < 4; ++mf) bst(rs, (unsigned)(OFF_PHIQ + (((size_t)(mt * 4 + mf) * 32 + h) * 64 + l) * 16), o[mf]);
            TSC(4);
            DRAIN();
            TSC(5);
            if (l == 0) {
                const unsigned old = __hip_atomic_fetch_add((unsigned*)&sFlag[2], 1u, __ATOMIC_RELAXED, __HIP_MEMORY_SCOPE_WORKGROUP);
                if ((old & 15u) == 3u) {
                    __hip_atomic_fetch_add(cnt + (CNT_CP + mt) * 64, 1u, RLX_AGENT);
                    if (wunit) __hip_atomic_fetch_add(cnt + (CNT_CW + (blockIdx.x >> 4)) * 64, 1u, RLX_AGENT);
                }
            }
            TSA(1);
        }
    }
    TSA(2);
    if (wv == 7) { wait_cnt(cnt + (CNT_CW + ng) * 64, ng == 10 ? 12u : 16u); wait_cnt(cnt + (CNT_CP + mt2) * 64, 8u); if (l == 0) sFlag[0] = 1u; }
    else { unsigned sp = 0; while (sFlag[0] == 0u) { __builtin_amdgcn_s_sleep(1); if (++sp > (1u << 22)) break; } }
    asm volatile("" ::: "memory");
    TSA(4);
    u32x4 ph[4][2][2];
#pragma unroll
    for (int hp = 0; hp < 4; ++hp)
#pragma unroll
        for (int hh = 0; hh < 2; ++hh)
#pragma unroll
            for (int mf = 0; mf < 2; ++mf)
                ph[hp][hh][mf] = bld(rs, (unsigned)(OFF_PHIQ + (((size_t)(mt2 * 4 + 2 * mh + mf) * 32 + kq * 8 + 2 * hp + hh) * 64 + l) * 16));
    SB();
    const int fmf = 2 * mh + (kq >> 1), fp = kq & 1;
    const int tok2 = (mt2 * 4 + fmf) * 16 + (l & 15);
    const int n02 = nt2 * 64 + fp * 32 + g * 8;
    const u32x4 xv2 = *(const u32x4*)(x + (size_t)tok2 * D_ + n02);
    const u32x4 gv2 = *(const u32x4*)((const u16*)(ws + OFF_MOD) + 1024 + n02);
    SB();
    if (wv != 7 && wv != 0) {
#pragma unroll
        for (int i = 0; i < B3_EARLY; ++i) K3_LOADB(i);
    }
    if (wv == 7) {
        wait_cnt(cnt + (CNT_CV + 16 + (bi & 15)) * 64, 1u);
        if (l == 0) sFlag[1] = 1u;
    } else { unsigned sp = 0; while (sFlag[1] == 0u) { __builtin_amdgcn_s_sleep(1); if (++sp > (1u << 22)) break; } }
    asm volatile("" ::: "memory");
    TSA(5);
    {
        u32x4 kt[4], zt;
#pragma unroll
        for (int i = 0; i < 4; ++i) kt[i] = bld(rs, (unsigned)(OFF_KVF + ((wv * 4 + i) * 64 + l) * 16));
        zt = bld(rs, (unsigned)(OFF_ZF + (wv * 16 + (l & 15)) * 16));
        SB();
        if (wv == 7 || wv == 0) {
#pragma unroll
            for (int i = 0; i < B3_EARLY; ++i) K3_LOADB(i);
        }
#pragma unroll
        for (int i = B3_EARLY; i < B3_MID; ++i) K3_LOADB(i);
        { unsigned sp = 0; while (sFlag[2] != 68u) { __builtin_amdgcn_s_sleep(1); if (++sp > (1u << 22)) break; } }
        asm volatile("" ::: "memory");
#pragma unroll
        for (int i = 0; i < 4; ++i) smem[(wv * 4 + i) * 64 + l] = kt[i];
        if (l < 16) sZ[wv * 16 + l] = zt;
    }
    __syncthreads();
    TS(6);
    {
        f32x4 acc[4][2];
#pragma unroll
        for (int a = 0; a < 4; ++a) { acc[a][0] = zero; acc[a][1] = zero; }
#pragma unroll
        for (int hp = 0; hp < 4; ++hp) {
            u32x4 of[2];
#pragma unroll
            for (int hh = 0; hh < 2; ++hh) {
                const int h = kq * 8 + 2 * hp + hh;
                const u32x4 ka = smem[h * 64 + l], za = sZ[h * 4 + g];
#pragma unroll
                for (int mf = 0; mf < 2; ++mf) {
                    const f32x4 num = MFMA32(ka, ph[hp][hh][mf], zero);
                    const f32x4 den = MFMA32(za, ph[hp][hh][mf], zero);
                    const float rd = frcp(den[0] + 1e-6f);
                    of[mf][2 * hh] = pk_bf16(num[0] * rd, num[1] * rd); of[mf][2 * hh + 1] = pk_bf16(num[2] * rd, num[3] * rd);
                }
            }
#pragma unroll
            for (int nf = 0; nf < 4; ++nf) {
                const u32x4 wf = sWO[(nf * 16 + kq * 4 + hp) * 64 + l];
#pragma unroll
                for (int mf = 0; mf < 2; ++mf) acc[nf][mf] = MFMA32(wf, of[mf], acc[nf][mf]);
            }
        }
        TSB(1);
        __syncthreads();
        f32x4* sR = (f32x4*)smem;
#pragma unroll
        for (int nf = 0; nf < 4; ++nf)
#pragma unroll
            for (int mf = 0; mf < 2; ++mf) sR[(wv * 8 + nf * 2 + mf) * 64 + l] = acc[nf][mf];
        __syncthreads();
        TSB(2);
        f32x4 a0 = zero, a1 = zero;
#pragma unroll
        for (int ww = 0; ww < 4; ++ww) {
            a0 += sR[((mh * 4 + ww) * 8 + (2 * fp) * 2 + (kq >> 1)) * 64 + l];
            a1 += sR[((mh * 4 + ww) * 8 + (2 * fp + 1) * 2 + (kq >> 1)) * 64 + l];
        }
        u32x4 o;
        o[0] = pk_bf16(bf_lo(xv2[0]) + bf_lo(gv2[0]) * a0[0], bf_hi(xv2[0]) + bf_hi(gv2[0]) * a0[1]);
        o[1] = pk_bf16(bf_lo(xv2[1]) + bf_lo(gv2[1]) * a0[2], bf_hi(xv2[1]) + bf_hi(gv2[1]) * a0[3]);
        o[2] = pk_bf16(bf_lo(xv2[2]) + bf_lo(gv2[2]) * a1[0], bf_hi(xv2[2]) + bf_hi(gv2[2]) * a1[1]);
        o[3] = pk_bf16(bf_lo(xv2[3]) + bf_lo(gv2[3]) * a1[2], bf_hi(xv2[3]) + bf_hi(gv2[3]) * a1[3]);
        bst(rs, (unsigned)(OFF_X1 + (((size_t)(mt2 * 4 + fmf) * 16 + (nt2 * 2 + fp)) * 64 + l) * 16), o);
        float vals[8];
#pragma unroll
        for (int i = 0; i < 4; ++i) { vals[2 * i] = bf_lo(o[i]); vals[2 * i + 1] = bf_hi(o[i]); }
        float s = ((vals[0] + vals[1]) + (vals[2] + vals[3])) + ((vals[4] + vals[5]) + (vals[6] + vals[7])), m, dm;
        red2_add(s, 0.f, m, dm);
        m *= (1.f / 32.f);
        float q = 0.f, Q;
#pragma unroll
        for (int i = 0; i < 8; ++i) { const float d = vals[i] - m; q += d * d; }
        red2_add(q, 0.f, Q, dm);
        if (g == 0) bst64(rs, (unsigned)(OFF_ST + (((size_t)(mt2 * 4 + fmf) * 16 + nt2 * 2 + fp) * 16 + (l & 15)) * 8), (u32x2){f2u(m), f2u(Q)});
        TSB(3);
        DRAIN();
        __syncthreads();
        if (threadIdx.x == 0) __hip_atomic_fetch_add(cnt + (CNT_CX + mt2) * 64, 1u, RLX_AGENT);
    }
    TSW(1);
#pragma unroll
    for (int i = B3_MID; i < 16; ++i) K3_LOADB(i);
    TSW(2);
    const u32x4* WD = (const u32x4*)(ws + OFF_WD) + ((size_t)nt2 * 4 * KS_F + kq) * 64 + l;
    u32x4 wd[11][4];
    const int last4 = kq < 3 ? 40 : 36;
#define K4_LOADW(j) do { const int i_ = (j) >> 2, f_ = (j) & 3; wd[i_][f_] = WD[f_ * KS_F * 64 + (i_ < 10 ? 4 * i_ : last4) * 64]; } while (0)
    if (act3) {
        u32x4* sA = smem;
        if (wv == 7) {
            const int ma = (mf0 * 16) >> 6, mb = (mf0 * 16 + nmf * 16 - 1) >> 6;
            wait_cnt(cnt + (CNT_CX + ma) * 64, 8u);
            wait_cnt(cnt + (CNT_CX + mb) * 64, 8u);
        }
        __syncthreads();
        TSW(3);
        u32x4 xa[4][3];
        u32x2 stp[4];
#define K3_LDA(c) do { _Pragma("unroll") for (int i = 0; i < 3; ++i) { const int mf = 3 * mh + i, mfa = mf0 + (mf < nmf ? mf : 0); \
                xa[c][i] = bld(rs, (unsigned)(OFF_X1 + (((size_t)mfa * 16 + 4 * (c) + kq) * 64 + l) * 16)); } SB(); } while (0)
#define K3_WRA(c) do { _Pragma("unroll") for (int i = 0; i < 3; ++i) sA[((3 * mh + i) * 16 + 4 * (c) + kq) * 64 + l] = xa[c][i]; SB(); } while (0)
#define K3_RDA(buf, ks) do { _Pragma("unroll") for (int mf = 0; mf < 6; ++mf) af[buf][mf] = sA[(mf * 16 + (ks)) * 64 + l]; } while (0)
        K3_LDA(0); K3_LDA(1); K3_LDA(2); K3_LDA(3);
        K3_WRA(0); K3_WRA(1); K3_WRA(2); K3_WRA(3);
        __syncthreads(); SB();
        TSB(4);
        f32x4 ag[6], au[6];
#pragma unroll
        for (int mf = 0; mf < 6; ++mf) { ag[mf] = zero; au[mf] = zero; }
        u32x4 af[2][6];
        f32x4 c1g, c2g, c1u, c2u;
        K3_RDA(0, 0);
#pragma unroll
        for (int ks = 0; ks < 16; ++ks) {
            if (ks == 2) {
                const int i = wv < 6 ? wv : 0, mfa = mf0 + (i < nmf ? i : 0);
#pragma unroll
                for (int j = 0; j < 4; ++j) stp[j] = bld64(rs, (unsigned)(OFF_ST + (((size_t)mfa * 16 + 4 * g + j) * 16 + (l & 15)) * 8));
                SB();
            }
            if (ks == 9) {
                float m, dummy, q;
                const float m0 = u2f(stp[0][0]), m1 = u2f(stp[1][0]), m2 = u2f(stp[2][0]), m3 = u2f(stp[3][0]);
                red2_add((m0 + m1) + (m2 + m3), 0.f, m, dummy);
                m *= 0.0625f;
                const float e0 = m0 - m, e1 = m1 - m, e2 = m2 - m, e3 = m3 - m;
                red2_add(((u2f(stp[0][1]) + u2f(stp[1][1])) + (u2f(stp[2][1]) + u2f(stp[3][1]))) + 32.f * ((e0 * e0 + e1 * e1) + (e2 * e2 + e3 * e3)), 0.f, q, dummy);
                if (g == 0 && wv < 6) sSt[wv * 16 + l] = (f32x2){rsqrtf(q * (1.f / D_) + 1e-5f), -m};
                SB();
            }
            if (ks == 11) { __syncthreads(); SB(); }
            if (ks == 12) {
                const unsigned co = (unsigned)(OFF_C12 + (size_t)(actw ? pair : ng * 8) * 256 + g * 16);
                c1g = __builtin_bit_cast(f32x4, bld(rs, co)); c2g = __builtin_bit_cast(f32x4, bld(rs, co + 64));
                c1u = __builtin_bit_cast(f32x4, bld(rs, co + 128)); c2u = __builtin_bit_cast(f32x4, bld(rs, co + 192)); SB();
            }
            if (ks + 1 < 16) K3_RDA((ks + 1) & 1, ks + 1);
            SB();
#pragma unroll
            for (int mf = 0; mf < 6; ++mf) { ag[mf] = MFMA32(bg[ks], af[ks & 1][mf], ag[mf]); au[mf] = MFMA32(bu[ks], af[ks & 1][mf], au[mf]); }
            SB();
            if (ks >= 3 && ks < 15) { K4_LOADW(2 * (ks - 3)); K4_LOADW(2 * (ks - 3) + 1); SB(); }
        }
        TSW(4);
        if (actw) {
#pragma unroll
            for (int mf = 0; mf < 6; ++mf) {
                if (mf < nmf) {
                    const f32x2 st = sSt[mf * 16 + (l & 15)];
                    float hv[4];
#pragma unroll
                    for (int j = 0; j < 4; ++j) {
                        const float gq = (ag[mf][j] + st[1] * c1g[j]) * st[0] + c2g[j];
                        const float uq = (au[mf][j] + st[1] * c1u[j]) * st[0] + c2u[j];
                        hv[j] = gelu_tanh(gq) * uq;
                    }
                    bst64(rs, (unsigned)(OFF_HH + ((size_t)(mf0 + mf) * KS_F + (pair >> 1)) * 1024 + l * 16 + (pair & 1) * 8), (u32x2){pk_bf16(hv[0], hv[1]), pk_bf16(hv[2], hv[3])});
                }
            }
        }
        TSB(5);
        DRAIN();
        __syncthreads();
        if (threadIdx.x == 0) __hip_atomic_fetch_add(cnt + (CNT_CH + mt3) * 64, 1u, RLX_AGENT);
    } else {
#pragma unroll
        for (int j = 0; j < 24; ++j) K4_LOADW(j);
    }
    TSW(5);
#pragma unroll
    for (int j = 24; j < 44; ++j) K4_LOADW(j);
    SB();
    {
        if (wv == 7) {
            const int ma = (mt2 * 64) / 96, mb = (mt2 * 64 + 63) / 96;
            wait_cnt(cnt + (CNT_CH + ma) * 64, 11u);
            wait_cnt(cnt + (CNT_CH + mb) * 64, 11u);
        }
        __syncthreads();
        TSW(6);
        f32x4 acc[4][2];
#pragma unroll
        for (int a = 0; a < 4; ++a) { acc[a][0] = zero; acc[a][1] = zero; }
        u32x4 hf[11][2];
#define K4_LOADH(i) do { _Pragma("unroll") for (int mf = 0; mf < 2; ++mf) \
            hf[i][mf] = bld(rs, (unsigned)(OFF_HH + (((size_t)(mt2 * 4 + 2 * mh + mf) * KS_F + kq + ((i) < 10 ? 4 * (i) : last4)) * 64 + l) * 16)); SB(); } while (0)
#pragma unroll
        for (int i = 0; i < 3; ++i) K4_LOADH(i);
        const u32x4 xv = bld(rs, (unsigned)(OFF_X1 + (((size_t)(mt2 * 4 + fmf) * 16 + (nt2 * 2 + fp)) * 64 + l) * 16));
        const u32x4 gv = *(const u32x4*)((const u16*)(ws + OFF_MOD) + 2560 + n02);
        SB();
#pragma unroll
        for (int i = 0; i < 11; ++i) {
            if (i + 3 < 11) K4_LOADH(i + 3);
            if (i < 10 || kq < 3) {
#pragma unroll
                for (int nf = 0; nf < 4; ++nf)
#pragma unroll
                    for (int mf = 0; mf < 2; ++mf) acc[nf][mf] = MFMA32(wd[i][nf], hf[i][mf], acc[nf][mf]);
            }
            SB();
        }
        f32x4* sR = (f32x4*)smem;
#pragma unroll
        for (int nf = 0; nf < 4; ++nf)
#pragma unroll
            for (int mf = 0; mf < 2; ++mf) sR[(wv * 8 + nf * 2 + mf) * 64 + l] = acc[nf][mf];
        __syncthreads();
        f32x4 a0 = zero, a1 = zero;
#pragma unroll
        for (int ww = 0; ww < 4; ++ww) {
            a0 += sR[((mh * 4 + ww) * 8 + (2 * fp) * 2 + (kq >> 1)) * 64 + l];
            a1 += sR[((mh * 4 + ww) * 8 + (2 * fp + 1) * 2 + (kq >> 1)) * 64 + l];
        }
        u32x4 o;
        o[0] = pk_bf16(bf_lo(xv[0]) + bf_lo(gv[0]) * a0[0], bf_hi(xv[0]) + bf_hi(gv[0]) * a0[1]);
        o[1] = pk_bf16(bf_lo(xv[1]) + bf_lo(gv[1]) * a0[2], bf_hi(xv[1]) + bf_hi(gv[1]) * a0[3]);
        o[2] = pk_bf16(bf_lo(xv[2]) + bf_lo(gv[2]) * a1[0], bf_hi(xv[2]) + bf_hi(gv[2]) * a1[1]);
        o[3] = pk_bf16(bf_lo(xv[3]) + bf_lo(gv[3]) * a1[2], bf_hi(xv[3]) + bf_hi(gv[3]) * a1[3]);
        *(u32x4*)(out + (size_t)tok2 * D_ + n02) = o;
    }
    TSW_FLUSH(2);
    TS_FLUSH(1);
}

extern "C" __global__ void kf_end_marker() {}

extern "C" void kernel_launch(void* const* d_in, const int* in_sizes, int n_in,
                              void* d_out, int out_size, void* d_ws, size_t ws_size,
                              hipStream_t stream) {
    const u16* x = (const u16*)d_in[0];
    const u16* wqkv = (const u16*)d_in[1];
    const u16* wphi = (const u16*)d_in[2];
    const u16* wo = (const u16*)d_in[3];
    const u16* wg = (const u16*)d_in[5];
    const u16* wu = (const u16*)d_in[6];
    const u16* wd = (const u16*)d_in[7];
    const u16* cvec = (const u16*)d_in[8];
    const u16* wmod = (const u16*)d_in[9];
    char* ws = (char*)d_ws;
    k0_prep<<<K0_GRID, 256, 0, stream>>>(x, wqkv, wphi, cvec, wmod, wo, wg, wu, wd, ws);
    kf_main<<<256, 512, 0, stream>>>(x, ws, (u16*)d_out);
}
```

```cpp
#include <hip/hip_runtime.h>
#include <hip/hip_bf16.h>
#include <stdint.h>

typedef short bf16x8 __attribute__((ext_vector_type(8)));
typedef short bf16x4 __attribute__((ext_vector_type(4)));
typedef float f32x4 __attribute__((ext_vector_type(4)));
typedef float f32x2 __attribute__((ext_vector_type(2)));
typedef unsigned u32x4 __attribute__((ext_vector_type(4)));
typedef unsigned u32x2 __attribute__((ext_vector_type(2)));
typedef __bf16 bf16x2_t __attribute__((ext_vector_type(2)));
typedef unsigned short u16;

#define PT_ST 2
#define K3_LA 4
#define DI __device__ __forceinline__
#define MFMA32(a, b, c) __builtin_amdgcn_mfma_f32_16x16x32_bf16(__builtin_bit_cast(bf16x8, (a)), __builtin_bit_cast(bf16x8, (b)), (c), 0, 0, 0)
#define SB() __builtin_amdgcn_sched_barrier(0)

constexpr int S_ = 2048, D_ = 512, H_ = 32, F_ = 1376;
constexpr int NMF = S_ / 16;
constexpr int KS_D = D_ / 32;
constexpr int KS_F = F_ / 32;
constexpr int NPAIR = F_ / 16;
constexpr float LOG2E = 1.4426950408889634f;

constexpr size_t OFF_MOD  = 0;
constexpr size_t OFF_CNT  = 8192;
constexpr size_t OFF_KVF  = OFF_CNT + 49152;
constexpr size_t OFF_ZF   = OFF_KVF + 65536;
constexpr size_t OFF_SLAB = OFF_ZF + 4096;
constexpr size_t OFF_ZSL  = OFF_SLAB + (size_t)32 * 32 * 1024;
constexpr size_t OFF_XN   = OFF_ZSL + (size_t)32 * 32 * 128;
constexpr size_t OFF_WQKV = OFF_XN + (size_t)S_ * D_ * 2;
constexpr size_t OFF_WO   = OFF_WQKV + (size_t)96 * 16384;
constexpr size_t OFF_WGU  = OFF_WO + (size_t)32 * 16384;
constexpr size_t OFF_WD   = OFF_WGU + (size_t)88 * 2 * 16384;
constexpr size_t OFF_PHIQ = OFF_WD + (size_t)32 * KS_F * 1024;
constexpr size_t OFF_X1   = OFF_PHIQ + (size_t)NMF * 32 * 1024;
constexpr size_t OFF_ST   = OFF_X1 + (size_t)S_ * D_ * 2;
constexpr size_t OFF_C12  = OFF_ST + (size_t)S_ * 16 * 8;
constexpr size_t OFF_WGU2 = OFF_C12 + (size_t)88 * 1024;
constexpr size_t OFF_HH   = OFF_WGU2 + (size_t)88 * 2 * 16384;

#define TS_DECL
#define TS(idx)
#define TS_FLUSH(kid)
#define TSW(idx)
#define TSW_FLUSH(kid)

DI unsigned pk_bf16(float a, float b) {
    f32x2 v = {a, b};
    bf16x2_t r = __builtin_convertvector(v, bf16x2_t);
    return __builtin_bit_cast(unsigned, r);
}
DI float bf_lo(unsigned u) { return __uint_as_float(u << 16); }
DI float bf_hi(unsigned u) { return __uint_as_float(u & 0xffff0000u); }
DI float bf_round(float a) { return bf_lo(pk_bf16(a, 0.f)); }
DI float fexp2(float x) { return __builtin_amdgcn_exp2f(x); }
DI float frcp(float x) { return __builtin_amdgcn_rcpf(x); }

template <int KMAP, int NMAP>
DI void pretile_wave(const u16* __restrict__ W, int ldw, int ncols, int ks, int cgrp, u32x4* __restrict__ dst, int nks, int nf_mul, int nf_add,
                     u32x4* sm  ) {
    const int l = threadIdx.x & 63, g = l >> 4, c8 = l & 15;
    const int col0 = cgrp * 128 + c8 * 8;
    const bool ok = col0 < ncols;
    const int colc = ok ? col0 : 0;
    u32x4 r[8];
#pragma unroll
    for (int j = 0; j < 8; ++j) {
        const int k = ks * 32 + (KMAP == 0 ? 8 * g + j : 16 * (j >> 2) + 4 * g + (j & 3));
        r[j] = *(const u32x4*)(W + (size_t)k * ldw + colc);
    }
#pragma unroll
    for (int c = 0; c < 8; ++c) {
        u32x4 o;
#pragma unroll
        for (int jj = 0; jj < 4; ++jj) {
            const unsigned a = r[2 * jj][c >> 1], b = r[2 * jj + 1][c >> 1];
            o[jj] = (c & 1) ? ((a >> 16) | (b & 0xffff0000u)) : ((a & 0xffffu) | (b << 16));
        }
        const int n = c8 * 8 + c;
        int nf, rr;
        if (NMAP == 0) { nf = n >> 4; rr = n & 15; }
        else { const int cc = n & 31; nf = 2 * (n >> 5) + ((cc >> 2) & 1); rr = 4 * (cc >> 3) + (cc & 3); }
        sm[nf * 65 + g * 16 + rr] = o;
    }
    asm volatile("s_waitcnt lgkmcnt(0)" ::: "memory");
    __builtin_amdgcn_wave_barrier();
#pragma unroll
    for (int nf = 0; nf < 8; ++nf) {
        const int nfg = cgrp * 8 + nf;
        if (nfg * 16 < ncols) {
            const u32x4 o = sm[nf * 65 + l];
            u32x4* p_ = &dst[((size_t)(nfg * nf_mul + nf_add) * nks + ks) * 64 + l];
            asm volatile("global_store_dwordx4 %0, %1, off sc1\n\ts_nop 1" :: "v"(p_), "v"(o) : "memory");
        }
    }
}

DI float u2f(unsigned u) { return __uint_as_float(u); }
DI unsigned f2u(float f) { return __float_as_uint(f); }
DI void red2_max(float a, float b, float& A, float& B) {
    auto r = __builtin_amdgcn_permlane32_swap(f2u(a), f2u(b), false, false);
    const float c = fmaxf(u2f(r[0]), u2f(r[1]));
    auto r2 = __builtin_amdgcn_permlane16_swap(f2u(c), f2u(c), false, false);
    const float d = fmaxf(u2f(r2[0]), u2f(r2[1]));
    auto r3 = __builtin_amdgcn_permlane32_swap(f2u(d), f2u(d), false, false);
    A = u2f(r3[0]); B = u2f(r3[1]);
}
DI void red2_add(float a, float b, float& A, float& B) {
    auto r = __builtin_amdgcn_permlane32_swap(f2u(a), f2u(b), false, false);
    const float c = u2f(r[0]) + u2f(r[1]);
    auto r2 = __builtin_amdgcn_permlane16_swap(f2u(c), f2u(c), false, false);
    const float d = u2f(r2[0]) + u2f(r2[1]);
    auto r3 = __builtin_amdgcn_permlane32_swap(f2u(d), f2u(d), false, false);
    A = u2f(r3[0]); B = u2f(r3[1]);
}

constexpr int K0_MOD = 96, K0_XN = 128, K0_FOLD = 64, K0_WV = 16, K0_WO = 16, K0_WG = 0, K0_WU = 0, K0_WD = 0;
constexpr int K0_B0 = K0_MOD, K0_B1 = K0_B0 + K0_XN, K0_B2 = K0_B1 + K0_FOLD, K0_B3 = K0_B2 + K0_WV;
constexpr int K0_B4 = K0_B3 + K0_WO, K0_B5 = K0_B4 + K0_WG, K0_B6 = K0_B5 + K0_WU, K0_B7 = K0_B6 + K0_WD;
constexpr int K0_GRID = K0_B7;

__global__ __launch_bounds__(256) void k0_prep(const u16* __restrict__ x, const u16* __restrict__ wqkv, const u16* __restrict__ wphi,
                                               const u16* __restrict__ cvec, const u16* __restrict__ wmod,
                                               const u16* __restrict__ wo, char* __restrict__ ws) {
    __shared__ __attribute__((aligned(16))) u32x4 smT[4 * 520];
    float* red = (float*)smT;
    TS_DECL; TS(0);
    const int pb = blockIdx.x, t = threadIdx.x;
    const int b = pb < 160 ? pb + 96 : (pb < 256 ? pb - 160 : pb);
    if (b >= K0_B0 && b < K0_B0 + 12) ((u32x4*)(ws + OFF_CNT))[(b - K0_B0) * 256 + t] = (u32x4){0u, 0u, 0u, 0u};
    if (b < K0_B0) {
        const int cb = (b & 7) * 12 + (b >> 3);
        const int cgp = t & 3, kg = t >> 2;
        float acc[8];
#pragma unroll
        for (int i = 0; i < 8; ++i) acc[i] = 0.f;
        u32x4 wv[8]; float sc[8];
        const unsigned c2 = ((const unsigned*)cvec)[t];
#pragma unroll
        for (int i = 0; i < 8; ++i) wv[i] = *(const u32x4*)(wmod + (size_t)(kg + 64 * i) * 3072 + cb * 32 + cgp * 8);
        SB();
        {
            const float c0 = bf_lo(c2), c1 = bf_hi(c2);
            *(f32x2*)(red + 256 + 2 * t) = (f32x2){bf_round(c0 / (1.f + expf(-c0))), bf_round(c1 / (1.f + expf(-c1)))};
        }
        __syncthreads();
#pragma unroll
        for (int i = 0; i < 8; ++i) sc[i] = red[256 + kg + 64 * i];
#pragma unroll
        for (int i = 0; i < 8; ++i) {
#pragma unroll
            for (int j = 0; j < 4; ++j) {
                acc[2 * j] += sc[i] * bf_lo(wv[i][j]);
                acc[2 * j + 1] += sc[i] * bf_hi(wv[i][j]);
            }
        }
#pragma unroll
        for (int j = 0; j < 8; ++j) {
            acc[j] += u2f(__builtin_amdgcn_update_dpp(0, f2u(acc[j]), 0x124, 0xf, 0xf, false));
            acc[j] += u2f(__builtin_amdgcn_update_dpp(0, f2u(acc[j]), 0x128, 0xf, 0xf, false));
        }
#pragma unroll
        for (int j = 0; j < 8; j += 2) red2_add(acc[j], acc[j + 1], acc[j], acc[j + 1]);
        if ((t & 63) < 4) {
#pragma unroll
            for (int j = 0; j < 8; ++j) red[(t >> 6) * 32 + cgp * 8 + j] = acc[j];
        }
        __syncthreads();
        if (t < 32) {
            float s = bf_round((red[t] + red[32 + t]) + (red[64 + t] + red[96 + t]));
            const int n = cb * 32 + t, row = n >> 9;
            if (row == 1 || row == 4) s = 1.f + s;
            ((u16*)(ws + OFF_MOD))[n] = (u16)(pk_bf16(s, 0.f) & 0xffffu);
        }
    } else if (b < K0_B1) {
        const int mf = b - K0_B0, r = t & 15, c = t >> 4;
        const u16* xr = x + (size_t)(mf * 16 + r) * D_;
        u32x4 v[4];
#pragma unroll
        for (int i = 0; i < 4; ++i) v[i] = *(const u32x4*)(xr + (c + 16 * i) * 8);
        float f[32];
        float s = 0.f;
#pragma unroll
        for (int i = 0; i < 4; ++i)
#pragma unroll
            for (int j = 0; j < 4; ++j) { f[i * 8 + 2 * j] = bf_lo(v[i][j]); f[i * 8 + 2 * j + 1] = bf_hi(v[i][j]); s += f[i * 8 + 2 * j] + f[i * 8 + 2 * j + 1]; }
        s += __shfl_xor(s, 16); s += __shfl_xor(s, 32);
        if ((t & 63) < 16) red[(t >> 6) * 16 + r] = s;
        __syncthreads();
        const float m = ((red[r] + red[16 + r]) + (red[32 + r] + red[48 + r])) * (1.f / D_);
        float q = 0.f;
#pragma unroll
        for (int i = 0; i < 32; ++i) { f[i] -= m; q += f[i] * f[i]; }
        q += __shfl_xor(q, 16); q += __shfl_xor(q, 32);
        if ((t & 63) < 16) red[64 + (t >> 6) * 16 + r] = q;
        __syncthreads();
        const float var = (red[64 + r] + red[80 + r]) + (red[96 + r] + red[112 + r]);
        const float rs = rsqrtf(var * (1.f / D_) + 1e-5f);
        u32x4* dst = (u32x4*)(ws + OFF_XN) + (size_t)mf * 16 * 64;
#pragma unroll
        for (int i = 0; i < 4; ++i) {
            u32x4 o;
#pragma unroll
            for (int j = 0; j < 4; ++j) o[j] = pk_bf16(f[i * 8 + 2 * j] * rs, f[i * 8 + 2 * j + 1] * rs);
            const int ch = c + 16 * i;
            dst[(ch >> 2) * 64 + (ch & 3) * 16 + r] = o;
        }
    } else if (b < K0_B2) {
        const int wv = (b - K0_B1) * 4 + (t >> 6);
        const int hq = wv >> 2, kq = wv & 3;
        const int l = t & 63, r = l & 15, g = l >> 4, h = hq & 31;
        bf16x4 bphi;
#pragma unroll
        for (int j = 0; j < 4; ++j) bphi[j] = (short)wphi[h * 256 + (4 * g + j) * 16 + r];
        char* dst = ws + OFF_WQKV + (size_t)hq * 16384;
        bf16x4 a[8];
#pragma unroll
        for (int i = 0; i < 8; ++i) a[i] = *(const bf16x4*)(wqkv + (size_t)((kq * 8 + i) * 16 + r) * 1536 + hq * 16 + 4 * g);
#pragma unroll
        for (int i = 0; i < 8; ++i) {
            const int rb = kq * 8 + i;
            f32x4 d = {0.f, 0.f, 0.f, 0.f};
            d = __builtin_amdgcn_mfma_f32_16x16x16bf16_1k(a[i], bphi, d, 0, 0, 0);
            u32x2 o;
            o[0] = pk_bf16(d[0] * LOG2E, d[1] * LOG2E);
            o[1] = pk_bf16(d[2] * LOG2E, d[3] * LOG2E);
            const int ks = rb >> 1, gp = 2 * (rb & 1) + (g >> 1);
            *(u32x2*)(dst + ks * 1024 + (gp * 16 + r) * 16 + (g & 1) * 8) = o;
        }
    } else {
        u32x4* sm = smT + (t >> 6) * 520;
        if (b < K0_B3) {
            const int id = (b - K0_B2) * 4 + (t >> 6);
            pretile_wave<0, 0>(wqkv + 1024, 1536, 512, id >> 2, id & 3, (u32x4*)(ws + OFF_WQKV), KS_D, 1, 64, sm);
        } else {
            const int id = (b - K0_B3) * 4 + (t >> 6);
            pretile_wave<1, 1>(wo, 512, 512, id >> 2, id & 3, (u32x4*)(ws + OFF_WO), KS_D, 1, 0, sm);
        }
    }
    TS_FLUSH(0);
}

DI float fmax2(float a, float b) { return __builtin_amdgcn_fmed3f(a, b, __builtin_inff()); }
DI float fmin2(float a, float b) { return __builtin_amdgcn_fmed3f(a, b, -__builtin_inff()); }
template <int N>
DI void softmax_pm(const f32x4 (&p)[N], u32x4 (&o)[N]) {
    float a[N], b[N];
#pragma unroll
    for (int i = 0; i < N; ++i) {
        a[i] = fmax2(fmax2(p[i][0], p[i][1]), fmax2(p[i][2], p[i][3]));
        b[i] = -fmin2(fmin2(p[i][0], p[i][1]), fmin2(p[i][2], p[i][3]));
    }
#pragma unroll
    for (int i = 0; i < N; ++i) { auto r = __builtin_amdgcn_permlane32_swap(f2u(a[i]), f2u(b[i]), false, false); a[i] = fmax2(u2f(r[0]), u2f(r[1])); }
#pragma unroll
    for (int i = 0; i < N; ++i) { auto r = __builtin_amdgcn_permlane16_swap(f2u(a[i]), f2u(a[i]), false, false); a[i] = fmax2(u2f(r[0]), u2f(r[1])); }
#pragma unroll
    for (int i = 0; i < N; ++i) { auto r = __builtin_amdgcn_permlane32_swap(f2u(a[i]), f2u(a[i]), false, false); a[i] = u2f(r[0]); b[i] = u2f(r[1]); }
    float ep[N][4], en[N][4];
#pragma unroll
    for (int i = 0; i < N; ++i)
#pragma unroll
        for (int j = 0; j < 4; ++j) { ep[i][j] = fexp2(p[i][j] - a[i]); en[i][j] = fexp2(-b[i] - p[i][j]); }
#pragma unroll
    for (int i = 0; i < N; ++i) { a[i] = (ep[i][0] + ep[i][1]) + (ep[i][2] + ep[i][3]); b[i] = (en[i][0] + en[i][1]) + (en[i][2] + en[i][3]); }
#pragma unroll
    for (int i = 0; i < N; ++i) { auto r = __builtin_amdgcn_permlane32_swap(f2u(a[i]), f2u(b[i]), false, false); a[i] = u2f(r[0]) + u2f(r[1]); }
#pragma unroll
    for (int i = 0; i < N; ++i) { auto r = __builtin_amdgcn_permlane16_swap(f2u(a[i]), f2u(a[i]), false, false); a[i] = frcp(u2f(r[0]) + u2f(r[1])); }
#pragma unroll
    for (int i = 0; i < N; ++i) { auto r = __builtin_amdgcn_permlane32_swap(f2u(a[i]), f2u(a[i]), false, false); a[i] = u2f(r[0]); b[i] = u2f(r[1]); }
#pragma unroll
    for (int i = 0; i < N; ++i) {
        o[i][0] = pk_bf16(ep[i][0] * a[i], ep[i][1] * a[i]); o[i][1] = pk_bf16(ep[i][2] * a[i], ep[i][3] * a[i]);
        o[i][2] = pk_bf16(en[i][0] * b[i], en[i][1] * b[i]); o[i][3] = pk_bf16(en[i][2] * b[i], en[i][3] * b[i]);
    }
}

DI u32x4 modulate8(const u32x4 xv, const float (&gg)[8], const float (&ss)[8]) {
    u32x4 o;
#pragma unroll
    for (int j = 0; j < 4; ++j)
        o[j] = pk_bf16(bf_lo(xv[j]) * gg[2 * j] + ss[2 * j], bf_hi(xv[j]) * gg[2 * j + 1] + ss[2 * j + 1]);
    return o;
}

#define TSD(i)
#define TSA(i) TS(i)
#define TSB(i)
#define TSC(i)
#define RLX_AGENT __ATOMIC_RELAXED, __HIP_MEMORY_SCOPE_AGENT
constexpr int CNT_CK = 0, CNT_CV = 32, CNT_CP = 64, CNT_CX = 96, CNT_CH = 128, CNT_CW = 150, CNT_CT = 170, CNT_CD = 172;
typedef __amdgpu_buffer_rsrc_t rsrc_t;
DI u32x4 bld(rsrc_t rs, unsigned off) { return __builtin_amdgcn_raw_buffer_load_b128(rs, off, 0, 16); }
DI u32x2 bld64(rsrc_t rs, unsigned off) { return __builtin_amdgcn_raw_buffer_load_b64(rs, off, 0, 16); }
DI void bst(rsrc_t rs, unsigned off, u32x4 v) { __builtin_amdgcn_raw_buffer_store_b128(v, rs, off, 0, 16); }
DI void bst64(rsrc_t rs, unsigned off, u32x2 v) { __builtin_amdgcn_raw_buffer_store_b64(v, rs, off, 0, 16); }
#define POLL_SLEEP 4
#define DRAIN() asm volatile("s_waitcnt vmcnt(0)" ::: "memory")
DI void wait_cnt(unsigned* c, unsigned n) {
    unsigned sp = 0;
    const unsigned lane = threadIdx.x & 63;
    unsigned* p = c + (lane < n ? lane : 0);
    while (!__all(__hip_atomic_load(p, RLX_AGENT) != 0u)) { __builtin_amdgcn_s_sleep(POLL_SLEEP); if (++sp > (1u << 18)) break; }
    __builtin_amdgcn_fence(__ATOMIC_ACQUIRE, "wavefront");
    asm volatile("" ::: "memory");
}
DI void set_flag(unsigned* c) { __hip_atomic_store(c, 1u, RLX_AGENT); }
DI float gelu_tanh(float g) {
    const float k0 = -2.f * 0.7978845608028654f * LOG2E, k1 = k0 * 0.044715f;
    const float a = g * (g * g * k1 + k0);
    return g * frcp(1.f + fexp2(a));
}
DI u32x4 scale8(const u32x4 v, const u32x4 gv) {
    u32x4 o;
#pragma unroll
    for (int j = 0; j < 4; ++j) o[j] = pk_bf16(bf_lo(v[j]) * bf_lo(gv[j]), bf_hi(v[j]) * bf_hi(gv[j]));
    return o;
}

#define B3_EARLY 10
#define B3_K 12
#define B3_A 16
#define B3_PER 0
#define B3_MID 12
extern "C" __global__ __launch_bounds__(512, 2) void kf_main(const u16* __restrict__ x, const u16* __restrict__ wg, const u16* __restrict__ wu, const u16* __restrict__ wd,
                                                                char* __restrict__ ws, u16* __restrict__ out) {
    __shared__ __attribute__((aligned(16))) u32x4 smem[9216 + 128 + 128 + 64 + 128 + 4];
    TS_DECL; TS(0);
    const int xcd = blockIdx.x & 7, bi = blockIdx.x >> 3;
    const int wv = __builtin_amdgcn_readfirstlane(threadIdx.x >> 6);
    const int l = threadIdx.x & 63, g = l >> 4;
    unsigned* cnt = (unsigned*)(ws + OFF_CNT);
    const rsrc_t rs = __builtin_amdgcn_make_buffer_rsrc(ws, 0, 0x4000000, 0x00020000);
    u32x4* sWO = smem + 5120;
    u32x4* sZ = smem + 9216;
    u32x4* sGB = smem + 9216 + 128;
    f32x2* sSt = (f32x2*)(smem + 9216 + 256);
    volatile unsigned* sFlag = (volatile unsigned*)(smem + 9216 + 448);
    const u32x4 ones = {0x3f803f80u, 0x3f803f80u, 0x3f803f80u, 0x3f803f80u};
    const f32x4 zero = {0.f, 0.f, 0.f, 0.f};

    const int mt2 = 4 * xcd + (bi >> 3), nt2 = bi & 7;
    const int kq = wv & 3, mh = wv >> 2;
    const int t0_ = (242 * xcd) >> 3, t1_ = (242 * (xcd + 1)) >> 3;
    const int id3 = t0_ + bi;
    const bool act3 = id3 < t1_;
    const int id3c = act3 ? id3 : t0_;
    const int ng = (id3c % 121) / 11, mt3 = 11 * (id3c / 121) + (id3c % 121) % 11;
    const int mf0 = mt3 * 6;
    const int nmf = (NMF - mf0) < 6 ? (NMF - mf0) : 6;
    const int pair = ng * 8 + wv;
    const bool actw = pair < NPAIR;
    const unsigned wgoff = (unsigned)(OFF_WGU2 + ((size_t)(actw ? pair : ng * 8) * 2048 + l) * 16);
    u32x4 bg[16], bu[16];
#define K3_LOADB(ks) do { bg[ks] = bld(rs, wgoff + (ks) * 1024); bu[ks] = bld(rs, wgoff + 16384 + (ks) * 1024); SB(); } while (0)

#define WO_DMA(part) do { const u32x4* WO_ = (const u32x4*)(ws + OFF_WO) + ((size_t)nt2 * 64 + (part) * 8) * 64 + l; \
        const unsigned ldsb_ = (unsigned)(size_t)(sWO + (part) * 8 * 64); \
        _Pragma("unroll") for (int i_ = 0; i_ < 8; ++i_) { unsigned keep_; \
            asm volatile("s_mov_b32 %0, m0\n\ts_mov_b32 m0, %2\n\ts_nop 0\n\tglobal_load_lds_dwordx4 %1, off\n\ts_mov_b32 m0, %0" : "=&s"(keep_) : "v"(WO_ + i_ * 64), "s"(ldsb_ + i_ * 1024) : "memory"); } } while (0)
    if (threadIdx.x < 16) sFlag[threadIdx.x] = 0u;
    {
        u32x4* sA = smem;
        char* sP = (char*)(smem + 4096);
        const int mt = 8 * (xcd >> 1) + (bi >> 2), hg = 4 * (xcd & 1) + (bi & 3);
        const int w = wv & 3;
        const int h = hg * 4 + w;
        const u32x4* WQ = (const u32x4*)(ws + OFF_WQKV) + (size_t)h * 1024 + l;
        u32x4 af[2][4];
#define K1_RDA(buf, ks) do { _Pragma("unroll") for (int mf = 0; mf < 4; ++mf) af[buf][mf] = sA[(mf * 16 + (ks)) * 64 + l]; } while (0)
        const u32x4* XN = (const u32x4*)(ws + OFF_XN) + (size_t)mt * 4096 + mh * 2048 + w * 64 + l;
        const u32x4* MS = (const u32x4*)(ws + OFF_MOD) + w * 4 + g;
        const u32x4* WB = WQ + (wv < 4 ? 32 : 64) * 1024;
        u32x4 xa[4][2], gs[4][2], bb[16];
#define K1_LOADA(c) do { gs[c][0] = MS[64 + (c) * 16]; gs[c][1] = MS[(c) * 16]; \
        _Pragma("unroll") for (int mf = 0; mf < 2; ++mf) xa[c][mf] = XN[mf * 1024 + (c) * 256]; SB(); } while (0)
#define K1_PREP1(c, mf) do { float gg[8], ss[8]; \
        _Pragma("unroll") for (int j = 0; j < 4; ++j) { gg[2 * j] = bf_lo(gs[c][0][j]); gg[2 * j + 1] = bf_hi(gs[c][0][j]); ss[2 * j] = bf_lo(gs[c][1][j]); ss[2 * j + 1] = bf_hi(gs[c][1][j]); } \
        sA[((2 * mh + (mf)) * 16 + 4 * (c) + w) * 64 + l] = modulate8(xa[c][mf], gg, ss); } while (0)
#define K1_LOADB(ks) do { bb[ks] = WB[(ks) * 64]; } while (0)
        f32x4 ac[4];
#pragma unroll
        for (int mf = 0; mf < 4; ++mf) ac[mf] = zero;
        K1_LOADA(0); K1_LOADA(1);
        K1_LOADB(0); K1_LOADB(1); K1_LOADB(2); K1_LOADB(3); SB();
        K1_LOADA(2); K1_LOADA(3);
        K1_LOADB(4); K1_LOADB(5); K1_LOADB(6); K1_LOADB(7); SB();
        K1_PREP1(0, 0); K1_PREP1(0, 1); K1_PREP1(1, 0); K1_PREP1(1, 1);
        __syncthreads(); SB();
        TSC(5);
        u32x4 bq[16];
        u32x4 a3[3][4];
#define K1_RD3(ks) do { _Pragma("unroll") for (int mf = 0; mf < 4; ++mf) a3[(ks) % 3][mf] = sA[(mf * 16 + (ks)) * 64 + l]; } while (0)
        K1_RD3(0); K1_RD3(1);
#pragma unroll
        for (int ks = 0; ks < 16; ++ks) {
            if (ks + 8 < 16) K1_LOADB(ks + 8);
            else if (wv >= 4) bq[ks - 8] = WQ[(ks - 8) * 64];
            if (ks == 6) { __syncthreads(); }
            if (ks + 2 < 16) K1_RD3(ks + 2);
#pragma unroll
            for (int mf = 0; mf < 4; ++mf) ac[mf] = MFMA32(bb[ks], a3[ks % 3][mf], ac[mf]);
            if (ks == 0) K1_PREP1(2, 0);
            if (ks == 1) K1_PREP1(2, 1);
            if (ks == 2) K1_PREP1(3, 0);
            if (ks == 3) K1_PREP1(3, 1);
            SB();
        }
        char* sV = (char*)(sWO + 3584);
        if (wv < 4) {
            __builtin_amdgcn_s_setprio(3);
            TSC(1);
            char* myP = sP + w * 4096;
            {
                u32x4 o[4];
                softmax_pm<4>(ac, o);
#pragma unroll
                for (int mf = 0; mf < 4; ++mf) {
                    char* row = myP + (mf * 16 + (l & 15)) * 64;
                    *(u32x2*)(row + g * 8) = (u32x2){o[mf][0], o[mf][1]};
                    *(u32x2*)(row + 32 + g * 8) = (u32x2){o[mf][2], o[mf][3]};
                }
            }
            TSC(2);
            const unsigned base = (unsigned)(size_t)myP + (4 * g + ((l & 15) >> 2)) * 64 + (l & 3) * 8;
            u32x2 t0, t1, t2, t3, t4, t5, t6, t7;
            asm volatile("s_waitcnt lgkmcnt(0)\n\t"
                         "ds_read_b64_tr_b16 %0, %8\n\tds_read_b64_tr_b16 %1, %8 offset:1024\n\t"
                         "ds_read_b64_tr_b16 %2, %8 offset:32\n\tds_read_b64_tr_b16 %3, %8 offset:1056\n\t"
                         "ds_read_b64_tr_b16 %4, %8 offset:2048\n\tds_read_b64_tr_b16 %5, %8 offset:3072\n\t"
                         "ds_read_b64_tr_b16 %6, %8 offset:2080\n\tds_read_b64_tr_b16 %7, %8 offset:3104\n\t"
                         "s_waitcnt lgkmcnt(0)"
                         : "=&v"(t0), "=&v"(t1), "=&v"(t2), "=&v"(t3), "=&v"(t4), "=&v"(t5), "=&v"(t6), "=&v"(t7) : "v"(base) : "memory");
            if (l == 0) __hip_atomic_fetch_add((unsigned*)&sFlag[2], 16u, __ATOMIC_RELAXED, __HIP_MEMORY_SCOPE_WORKGROUP);
            const u32x4 pa00 = {t0[0], t0[1], t1[0], t1[1]}, pa01 = {t2[0], t2[1], t3[0], t3[1]};
            const u32x4 pa10 = {t4[0], t4[1], t5[0], t5[1]}, pa11 = {t6[0], t6[1], t7[0], t7[1]};
            { unsigned sp = 0; while (sFlag[8 + w] == 0u) { __builtin_amdgcn_s_sleep(1); if (++sp > (1u << 22)) break; } }
            asm volatile("" ::: "memory");
            u32x4 vb0, vb1;
            {
                const unsigned vbase = (unsigned)(size_t)sV + w * 2048 + (4 * g + ((l & 15) >> 2)) * 32 + (l & 3) * 8;
                u32x2 v0, v1, v2, v3;
                asm volatile("ds_read_b64_tr_b16 %0, %4\n\tds_read_b64_tr_b16 %1, %4 offset:512\n\t"
                             "ds_read_b64_tr_b16 %2, %4 offset:1024\n\tds_read_b64_tr_b16 %3, %4 offset:1536\n\ts_waitcnt lgkmcnt(0)"
                             : "=&v"(v0), "=&v"(v1), "=&v"(v2), "=&v"(v3) : "v"(vbase) : "memory");
                vb0 = (u32x4){v0[0], v0[1], v1[0], v1[1]}; vb1 = (u32x4){v2[0], v2[1], v3[0], v3[1]};
            }
            f32x4 kv0 = MFMA32(pa00, vb0, zero), kv1 = MFMA32(pa01, vb0, zero);
            f32x4 z0 = MFMA32(pa00, ones, zero), z1 = MFMA32(pa01, ones, zero);
            kv0 = MFMA32(pa10, vb1, kv0); kv1 = MFMA32(pa11, vb1, kv1);
            z0 = MFMA32(pa10, ones, z0); z1 = MFMA32(pa11, ones, z1);
            u32x4 o;
            o[0] = pk_bf16(kv0[0], kv0[1]); o[1] = pk_bf16(kv0[2], kv0[3]); o[2] = pk_bf16(kv1[0], kv1[1]); o[3] = pk_bf16(kv1[2], kv1[3]);
            bst(rs, (unsigned)(OFF_SLAB + (((size_t)h * 32 + mt) * 64 + l) * 16), o);
            if ((l & 15) < 2) {
                const f32x4 zz = (l & 1) ? z1 : z0;
                bst(rs, (unsigned)(OFF_ZSL + (((size_t)h * 32 + mt) * 8 + g * 2 + (l & 1)) * 16), __builtin_bit_cast(u32x4, zz));
            }
            TSC(3);
            DRAIN();
            TSC(4);
            if (l == 0) set_flag(cnt + (CNT_CK + h) * 64 + mt);
            TSA(1);
            __builtin_amdgcn_s_setprio(0);
            if (l == 0) __hip_atomic_fetch_add((unsigned*)&sFlag[5], 1u, __ATOMIC_RELAXED, __HIP_MEMORY_SCOPE_WORKGROUP);
            const bool dunit = wv == 0 && blockIdx.x < 172;
            const bool wunit = (wv == 1 || wv == 2) && blockIdx.x >= 84;
            const int wmat = wv - 1, wcg = ((int)blockIdx.x - 84) >> 2, wkq = blockIdx.x & 3;
            u32x4 rr[1][8], wgam[4], wbet[4];
            if (dunit) {
                const int ks = blockIdx.x >> 2, cgrp = blockIdx.x & 3;
#pragma unroll
                for (int j = 0; j < 8; ++j) rr[0][j] = *(const u32x4*)(wd + (size_t)(ks * 32 + 16 * (j >> 2) + 4 * g + (j & 3)) * 512 + cgrp * 128 + (l & 15) * 8);
            }
            if (wunit) {
                const u16* W = wmat ? wu : wg;
#pragma unroll
                for (int j = 0; j < 8; ++j)
                    rr[0][j] = *(const u32x4*)(W + (size_t)((wkq * 4 + (l >> 4)) * 32 + 8 * ((l >> 2) & 3) + j) * F_ + wcg * 32 + (l & 3) * 8);
#pragma unroll
                for (int i = 0; i < 4; ++i) {
                    wgam[i] = ((const u32x4*)(ws + OFF_MOD))[256 + (wkq * 4 + i) * 4 + g];
                    wbet[i] = ((const u32x4*)(ws + OFF_MOD))[192 + (wkq * 4 + i) * 4 + g];
                }
            }
            SB();
            if (wv == 3) {
                { unsigned sp = 0; while (sFlag[5] != 4u) { __builtin_amdgcn_s_sleep(1); if (++sp > (1u << 22)) break; } }
                WO_DMA(7); WO_DMA(6); WO_DMA(5); WO_DMA(4);
            }
            if (dunit || wunit) {
                { unsigned sp = 0; while (sFlag[6] != 4u || (sFlag[2] >> 4) != 4u) { __builtin_amdgcn_s_sleep(1); if (++sp > (1u << 22)) break; } }
                asm volatile("" ::: "memory");
            }
            TSD(2);
            TSD(3);
            u32x4* sm = smem + w * 1040;
            if (dunit) {
                const int ks = blockIdx.x >> 2, cgrp = blockIdx.x & 3, c8 = l & 15;
#pragma unroll
                for (int c = 0; c < 8; ++c) {
                    u32x4 t_;
#pragma unroll
                    for (int jj = 0; jj < 4; ++jj) {
                        const unsigned a = rr[0][2 * jj][c >> 1], b = rr[0][2 * jj + 1][c >> 1];
                        t_[jj] = (c & 1) ? ((a >> 16) | (b & 0xffff0000u)) : ((a & 0xffffu) | (b << 16));
                    }
                    const int n = c8 * 8 + c, cc = n & 31;
                    sm[(2 * (n >> 5) + ((cc >> 2) & 1)) * 65 + g * 16 + 4 * (cc >> 3) + (cc & 3)] = t_;
                }
                asm volatile("s_waitcnt lgkmcnt(0)" ::: "memory");
                __builtin_amdgcn_wave_barrier();
#pragma unroll
                for (int nf = 0; nf < 8; ++nf) bst(rs, (unsigned)(OFF_WD + (((size_t)(cgrp * 8 + nf) * KS_F + ks) * 64 + l) * 16), sm[nf * 65 + l]);
                TSD(4);
                DRAIN();
                TSD(5);
                if (l == 0) set_flag(cnt + (CNT_CD + cgrp) * 64 + ks);
            }
            if (wunit) {
                const int k0 = wkq * 4;
#pragma unroll
                for (int c = 0; c < 8; ++c) {
                    u32x4 t_;
#pragma unroll
                    for (int jj = 0; jj < 4; ++jj) {
                        const unsigned a = rr[0][2 * jj][c >> 1], b = rr[0][2 * jj + 1][c >> 1];
                        t_[jj] = (c & 1) ? ((a >> 16) | (b & 0xffff0000u)) : ((a & 0xffffu) | (b << 16));
                    }
                    const int n = (l & 3) * 8 + c;
                    sm[((l >> 4) * 2 + (n >> 4)) * 65 + ((l >> 2) & 3) * 16 + (n & 15)] = t_;
                }
                asm volatile("s_waitcnt lgkmcnt(0)" ::: "memory");
                __builtin_amdgcn_wave_barrier();
                f32x4 c1[2], c2[2];
                c1[0] = zero; c1[1] = zero; c2[0] = zero; c2[1] = zero;
#pragma unroll
                for (int i = 0; i < 4; ++i)
#pragma unroll
                    for (int nf = 0; nf < 2; ++nf) {
                        const u32x4 v = sm[(i * 2 + nf) * 65 + l];
                        c2[nf] = MFMA32(v, wbet[i], c2[nf]);
                        const u32x4 o_ = scale8(v, wgam[i]);
                        c1[nf] = MFMA32(o_, ones, c1[nf]);
                        bst(rs, (unsigned)(OFF_WGU2 + (((size_t)((wcg * 2 + nf) * 2 + wmat) * 16 + k0 + i) * 64 + l) * 16), o_);
                    }
                if ((l & 15) < 4) {
                    const int nf = (l >> 1) & 1, which = l & 1;
                    const f32x4 cv = which ? (nf ? c2[1] : c2[0]) : (nf ? c1[1] : c1[0]);
                    bst(rs, (unsigned)(OFF_C12 + ((((size_t)(wcg * 2 + nf) * 4 + wkq) * 2 + wmat) * 2 + which) * 64 + g * 16), __builtin_bit_cast(u32x4, cv));
                }
                TSD(4);
                DRAIN();
                TSD(5);
                if (l == 0) set_flag(cnt + (CNT_CW + (wcg >> 2)) * 64 + ((wcg & 3) * 4 + wkq) * 2 + wmat);
            }
            if (wv == 0) { WO_DMA(0); WO_DMA(1); }
            if (wv == 1) { WO_DMA(2); }
            if (wv == 2) { WO_DMA(3); }
            DRAIN();
            if (wv == 3) {
                wait_cnt(cnt + (CNT_CW + ng) * 64, ng == 10 ? 24u : 32u); wait_cnt(cnt + (CNT_CD + (nt2 >> 1)) * 64, 43u);
                if (l == 0) sFlag[4] = 1u;
            } else { unsigned sp = 0; while (sFlag[4] == 0u) { __builtin_amdgcn_s_sleep(2); if (++sp > (1u << 22)) break; } }
            asm volatile("" ::: "memory");
            TSD(1);
#pragma unroll
            for (int i = 0; i < B3_K; ++i) K3_LOADB(i);
            TSA(3);
        } else {
            {
#pragma unroll
                for (int mf = 0; mf < 4; ++mf)
                    *(u32x2*)(sV + w * 2048 + (mf * 16 + (l & 15)) * 32 + g * 8) = (u32x2){pk_bf16(ac[mf][0], ac[mf][1]), pk_bf16(ac[mf][2], ac[mf][3])};
                asm volatile("s_waitcnt lgkmcnt(0)" ::: "memory");
                if (l == 0) sFlag[8 + w] = 1u;
            }
            SB();
#pragma unroll
            for (int i = 8; i < 16; ++i) bq[i] = WQ[i * 64];
            SB();
            TSC(1);
            f32x4 aq[4];
#pragma unroll
            for (int mf = 0; mf < 4; ++mf) aq[mf] = zero;
            K1_RD3(0); K1_RD3(1);
#pragma unroll
            for (int ks = 0; ks < 16; ++ks) {
                if (ks + 2 < 16) K1_RD3(ks + 2);
#pragma unroll
                for (int mf = 0; mf < 4; ++mf) aq[mf] = MFMA32(bq[ks], a3[ks % 3][mf], aq[mf]);
                SB();
            }
            TSC(3);
            if (l == 0) __hip_atomic_fetch_add((unsigned*)&sFlag[6], 1u, __ATOMIC_RELAXED, __HIP_MEMORY_SCOPE_WORKGROUP);
            SB();
            u32x4 o[4];
            softmax_pm<4>(aq, o);
            TSC(2);
            wait_cnt(cnt + (CNT_CK + h) * 64, 32u);
            TSD(1);
            {
                u32x4 sl[32], zl[4];
#pragma unroll
                for (int i = 0; i < 4; ++i) zl[i] = bld(rs, (unsigned)(OFF_ZSL + ((size_t)h * 256 + i * 64 + l) * 16));
#pragma unroll
                for (int i = 0; i < 32; ++i) sl[i] = bld(rs, (unsigned)(OFF_SLAB + (((size_t)h * 32 + i) * 64 + l) * 16));
                SB();
                TSD(2);
                float z[4];
#pragma unroll
                for (int j = 0; j < 4; ++j) z[j] = (u2f(zl[0][j]) + u2f(zl[1][j])) + (u2f(zl[2][j]) + u2f(zl[3][j]));
#pragma unroll
                for (int j = 0; j < 4; ++j) z[j] += __shfl_xor(z[j], 8);
                red2_add(z[0], z[1], z[0], z[1]); red2_add(z[2], z[3], z[2], z[3]);
                if (l < 8) ((u32x2*)sZ)[w * 8 + l] = (u32x2){pk_bf16(z[0], z[1]), pk_bf16(z[2], z[3])};
                float a[8];
#pragma unroll
                for (int j = 0; j < 4; ++j) { a[2 * j] = bf_lo(sl[0][j]); a[2 * j + 1] = bf_hi(sl[0][j]); }
#pragma unroll
                for (int i = 1; i < 32; ++i)
#pragma unroll
                    for (int j = 0; j < 4; ++j) { a[2 * j] += bf_lo(sl[i][j]); a[2 * j + 1] += bf_hi(sl[i][j]); }
                const u32x4 ka = (u32x4){pk_bf16(a[0], a[1]), pk_bf16(a[2], a[3]), pk_bf16(a[4], a[5]), pk_bf16(a[6], a[7])};
                asm volatile("s_waitcnt lgkmcnt(0)" ::: "memory");
                __builtin_amdgcn_wave_barrier();
                const u32x4 za = sZ[w * 4 + g];
                f32x4 num[4], den[4];
#pragma unroll
                for (int mf = 0; mf < 4; ++mf) { num[mf] = MFMA32(ka, o[mf], zero); den[mf] = MFMA32(za, o[mf], zero); }
#pragma unroll
                for (int mf = 0; mf < 4; ++mf) {
                    const float rd = frcp(den[mf][0] + 1e-6f);
                    bst64(rs, (unsigned)(OFF_PHIQ + (((size_t)(mt * 4 + mf) * 16 + (h >> 1)) * 64 + l) * 16 + (h & 1) * 8),
                          (u32x2){pk_bf16(num[mf][0] * rd, num[mf][1] * rd), pk_bf16(num[mf][2] * rd, num[mf][3] * rd)});
                }
            }
            TSC(4); TSD(3);
            DRAIN();
            TSC(5); TSD(4);
            if (l == 0) {
                const unsigned old = __hip_atomic_fetch_add((unsigned*)&sFlag[2], 1u, __ATOMIC_RELAXED, __HIP_MEMORY_SCOPE_WORKGROUP);
                if ((old & 15u) == 3u) set_flag(cnt + (CNT_CP + mt) * 64 + hg);
            }
            TSA(1);
            if (wv != 7) {
                { unsigned sp = 0; while (sFlag[4] == 0u) { __builtin_amdgcn_s_sleep(2); if (++sp > (1u << 22)) break; } }
                asm volatile("" ::: "memory");
#pragma unroll
                for (int i = 0; i < B3_K; ++i) K3_LOADB(i);
            }
        }
    }
    TSA(2);
    if (wv == 7) { wait_cnt(cnt + (CNT_CP + mt2) * 64, 8u); if (l == 0) sFlag[0] = 1u; }
    else { unsigned sp = 0; while (sFlag[0] == 0u) { __builtin_amdgcn_s_sleep(1); if (++sp > (1u << 22)) break; } }
    asm volatile("" ::: "memory");
    TSA(4);
    u32x4 of[4][2];
#pragma unroll
    for (int hp = 0; hp < 4; ++hp)
#pragma unroll
        for (int mf = 0; mf < 2; ++mf)
            of[hp][mf] = bld(rs, (unsigned)(OFF_PHIQ + (((size_t)(mt2 * 4 + 2 * mh + mf) * 16 + kq * 4 + hp) * 64 + l) * 16));
    SB();
    const int fmf = 2 * mh + (kq >> 1), fp = kq & 1;
    const int tok2 = (mt2 * 4 + fmf) * 16 + (l & 15);
    const int n02 = nt2 * 64 + fp * 32 + g * 8;
    const u32x4 xv2 = *(const u32x4*)(x + (size_t)tok2 * D_ + n02);
    const u32x4 gv2 = *(const u32x4*)((const u16*)(ws + OFF_MOD) + 1024 + n02);
    SB();
    TSA(5);
    __syncthreads();
    TS(6);
    {
        f32x4 acc[4][2];
#pragma unroll
        for (int a = 0; a < 4; ++a) { acc[a][0] = zero; acc[a][1] = zero; }
#pragma unroll
        for (int hp = 0; hp < 4; ++hp) {
#pragma unroll
            for (int i = hp * B3_PER; i < (hp + 1) * B3_PER; ++i) K3_LOADB(i);
#pragma unroll
            for (int nf = 0; nf < 4; ++nf) {
                const u32x4 wf = sWO[(nf * 16 + kq * 4 + hp) * 64 + l];
#pragma unroll
                for (int mf = 0; mf < 2; ++mf) acc[nf][mf] = MFMA32(wf, of[hp][mf], acc[nf][mf]);
            }
        }
        TSB(1);
        f32x4* sR = (f32x4*)smem;
#pragma unroll
        for (int nf = 0; nf < 4; ++nf)
#pragma unroll
            for (int mf = 0; mf < 2; ++mf) sR[(wv * 8 + nf * 2 + mf) * 64 + l] = acc[nf][mf];
        __syncthreads();
        TSB(2);
        f32x4 a0 = zero, a1 = zero;
#pragma unroll
        for (int ww = 0; ww < 4; ++ww) {
            a0 += sR[((mh * 4 + ww) * 8 + (2 * fp) * 2 + (kq >> 1)) * 64 + l];
            a1 += sR[((mh * 4 + ww) * 8 + (2 * fp + 1) * 2 + (kq >> 1)) * 64 + l];
        }
        u32x4 o;
        o[0] = pk_bf16(bf_lo(xv2[0]) + bf_lo(gv2[0]) * a0[0], bf_hi(xv2[0]) + bf_hi(gv2[0]) * a0[1]);
        o[1] = pk_bf16(bf_lo(xv2[1]) + bf_lo(gv2[1]) * a0[2], bf_hi(xv2[1]) + bf_hi(gv2[1]) * a0[3]);
        o[2] = pk_bf16(bf_lo(xv2[2]) + bf_lo(gv2[2]) * a1[0], bf_hi(xv2[2]) + bf_hi(gv2[2]) * a1[1]);
        o[3] = pk_bf16(bf_lo(xv2[3]) + bf_lo(gv2[3]) * a1[2], bf_hi(xv2[3]) + bf_hi(gv2[3]) * a1[3]);
        bst(rs, (unsigned)(OFF_X1 + (((size_t)(mt2 * 4 + fmf) * 16 + (nt2 * 2 + fp)) * 64 + l) * 16), o);
        float vals[8];
#pragma unroll
        for (int i = 0; i < 4; ++i) { vals[2 * i] = bf_lo(o[i]); vals[2 * i + 1] = bf_hi(o[i]); }
        float s = ((vals[0] + vals[1]) + (vals[2] + vals[3])) + ((vals[4] + vals[5]) + (vals[6] + vals[7])), m, dm;
        red2_add(s, 0.f, m, dm);
        m *= (1.f / 32.f);
        float q = 0.f, Q;
#pragma unroll
        for (int i = 0; i < 8; ++i) { const float d = vals[i] - m; q += d * d; }
        red2_add(q, 0.f, Q, dm);
        if (g == 0) bst64(rs, (unsigned)(OFF_ST + (((size_t)(mt2 * 4 + fmf) * 16 + nt2 * 2 + fp) * 16 + (l & 15)) * 8), (u32x2){f2u(m), f2u(Q)});
        TSB(3);
        DRAIN();
        __syncthreads();
        if (threadIdx.x == 0) set_flag(cnt + (CNT_CX + mt2) * 64 + nt2);
    }
    TSW(1);
    if (wv == 7) {
#pragma unroll
        for (int i = 0; i < B3_K; ++i) K3_LOADB(i);
    }
#pragma unroll
    for (int i = B3_K; i < B3_A; ++i) K3_LOADB(i);
    TSW(2);
    const unsigned wdoff = (unsigned)(OFF_WD + (((size_t)nt2 * 4 * KS_F + kq) * 64 + l) * 16);
    u32x4 wdf[11][4];
    const int last4 = kq < 3 ? 40 : 36;
#define K4_LOADW(j) do { const int i_ = (j) >> 2, f_ = (j) & 3; wdf[i_][f_] = bld(rs, wdoff + (f_ * KS_F + (i_ < 10 ? 4 * i_ : last4)) * 1024); } while (0)
    if (act3) {
        u32x4* sA = smem;
        if (wv == 6) {
            const int ma = (mf0 * 16) >> 6, mb = (mf0 * 16 + nmf * 16 - 1) >> 6;
            wait_cnt(cnt + (CNT_CX + ma) * 64, 8u);
            wait_cnt(cnt + (CNT_CX + mb) * 64, 8u);
        }
        __syncthreads();
        TSW(3);
        u32x4 xa[4][3];
        u32x2 stp[4];
#define K3_LDA(c) do { _Pragma("unroll") for (int i = 0; i < 3; ++i) { const int mf = 3 * mh + i, mfa = mf0 + (mf < nmf ? mf : 0); \
                xa[c][i] = bld(rs, (unsigned)(OFF_X1 + (((size_t)mfa * 16 + 4 * (c) + kq) * 64 + l) * 16)); } SB(); } while (0)
#define K3_WRA(c) do { _Pragma("unroll") for (int i = 0; i < 3; ++i) sA[((3 * mh + i) * 16 + 4 * (c) + kq) * 64 + l] = xa[c][i]; SB(); } while (0)
#define K3_RDA(buf, ks) do { _Pragma("unroll") for (int mf = 0; mf < 6; ++mf) af[buf][mf] = sA[(mf * 16 + (ks)) * 64 + l]; } while (0)
        K3_LDA(0); K3_LDA(1); K3_LDA(2); K3_LDA(3);
#pragma unroll
        for (int i = B3_A; i < 16; ++i) K3_LOADB(i);
        K3_WRA(0); K3_WRA(1); K3_WRA(2); K3_WRA(3);
        __syncthreads(); SB();
        TSB(4);
        f32x4 ag[6], au[6];
#pragma unroll
        for (int mf = 0; mf < 6; ++mf) { ag[mf] = zero; au[mf] = zero; }
        u32x4 af[2][6];
        f32x4 cpart;
        K3_RDA(0, 0);
#pragma unroll
        for (int ks = 0; ks < 16; ++ks) {
            if (ks == 2) {
                const int i = wv < 6 ? wv : 0, mfa = mf0 + (i < nmf ? i : 0);
#pragma unroll
                for (int j = 0; j < 4; ++j) stp[j] = bld64(rs, (unsigned)(OFF_ST + (((size_t)mfa * 16 + 4 * g + j) * 16 + (l & 15)) * 8));
                SB();
            }
            if (ks == 9) {
                float m, dummy, q;
                const float m0 = u2f(stp[0][0]), m1 = u2f(stp[1][0]), m2 = u2f(stp[2][0]), m3 = u2f(stp[3][0]);
                red2_add((m0 + m1) + (m2 + m3), 0.f, m, dummy);
                m *= 0.0625f;
                const float e0 = m0 - m, e1 = m1 - m, e2 = m2 - m, e3 = m3 - m;
                red2_add(((u2f(stp[0][1]) + u2f(stp[1][1])) + (u2f(stp[2][1]) + u2f(stp[3][1]))) + 32.f * ((e0 * e0 + e1 * e1) + (e2 * e2 + e3 * e3)), 0.f, q, dummy);
                if (g == 0 && wv < 6) sSt[wv * 16 + l] = (f32x2){rsqrtf(q * (1.f / D_) + 1e-5f), -m};
                SB();
            }
            if (ks == 11) { __syncthreads(); SB(); }
            if (ks == 12) {
                const unsigned co = (unsigned)(OFF_C12 + (size_t)(actw ? pair : ng * 8) * 1024 + (l & 15) * 64 + g * 16);
                cpart = __builtin_bit_cast(f32x4, bld(rs, co)); SB();
            }
            if (ks + 1 < 16) K3_RDA((ks + 1) & 1, ks + 1);
            SB();
#pragma unroll
            for (int mf = 0; mf < 6; ++mf) { ag[mf] = MFMA32(bg[ks], af[ks & 1][mf], ag[mf]); au[mf] = MFMA32(bu[ks], af[ks & 1][mf], au[mf]); }
            SB();
            if (ks >= 3 && ks < 15) { K4_LOADW(2 * (ks - 3)); K4_LOADW(2 * (ks - 3) + 1); SB(); }
        }
        TSW(4);
        f32x4 c1g, c2g, c1u, c2u;
#pragma unroll
        for (int j = 0; j < 4; ++j) {
            float v = cpart[j];
            v += u2f(__builtin_amdgcn_update_dpp(0, f2u(v), 0x124, 0xf, 0xf, false));
            v += u2f(__builtin_amdgcn_update_dpp(0, f2u(v), 0x128, 0xf, 0xf, false));
            c1g[j] = u2f(__builtin_amdgcn_update_dpp(0, f2u(v), 0x00, 0xf, 0xf, false));
            c2g[j] = u2f(__builtin_amdgcn_update_dpp(0, f2u(v), 0x55, 0xf, 0xf, false));
            c1u[j] = u2f(__builtin_amdgcn_update_dpp(0, f2u(v), 0xaa, 0xf, 0xf, false));
            c2u[j] = u2f(__builtin_amdgcn_update_dpp(0, f2u(v), 0xff, 0xf, 0xf, false));
        }
        if (actw) {
#pragma unroll
            for (int mf = 0; mf < 6; ++mf) {
                if (mf < nmf) {
                    const f32x2 st = sSt[mf * 16 + (l & 15)];
                    float hv[4];
#pragma unroll
                    for (int j = 0; j < 4; ++j) {
                        const float gq = (ag[mf][j] + st[1] * c1g[j]) * st[0] + c2g[j];
                        const float uq = (au[mf][j] + st[1] * c1u[j]) * st[0] + c2u[j];
                        hv[j] = gelu_tanh(gq) * uq;
                    }
                    bst64(rs, (unsigned)(OFF_HH + ((size_t)(mf0 + mf) * KS_F + (pair >> 1)) * 1024 + l * 16 + (pair & 1) * 8), (u32x2){pk_bf16(hv[0], hv[1]), pk_bf16(hv[2], hv[3])});
                }
            }
        }
        TSB(5);
        DRAIN();
        __syncthreads();
        if (threadIdx.x == 0) set_flag(cnt + (CNT_CH + mt3) * 64 + ng);
    } else {
#pragma unroll
        for (int j = 0; j < 24; ++j) K4_LOADW(j);
    }
    TSW(5);
#pragma unroll
    for (int j = 24; j < 44; ++j) K4_LOADW(j);
    SB();
    {
        if (wv == 7) {
            const int ma = (mt2 * 64) / 96, mb = (mt2 * 64 + 63) / 96;
            wait_cnt(cnt + (CNT_CH + ma) * 64, 11u);
            wait_cnt(cnt + (CNT_CH + mb) * 64, 11u);
        }
        __syncthreads();
        TSW(6);
        f32x4 acc[4][2];
#pragma unroll
        for (int a = 0; a < 4; ++a) { acc[a][0] = zero; acc[a][1] = zero; }
        u32x4 hf[11][2];
#define K4_LOADH(i) do { _Pragma("unroll") for (int mf = 0; mf < 2; ++mf) \
            hf[i][mf] = bld(rs, (unsigned)(OFF_HH + (((size_t)(mt2 * 4 + 2 * mh + mf) * KS_F + kq + ((i) < 10 ? 4 * (i) : last4)) * 64 + l) * 16)); SB(); } while (0)
#pragma unroll
        for (int i = 0; i < 3; ++i) K4_LOADH(i);
        const u32x4 xv = bld(rs, (unsigned)(OFF_X1 + (((size_t)(mt2 * 4 + fmf) * 16 + (nt2 * 2 + fp)) * 64 + l) * 16));
        const u32x4 gv = *(const u32x4*)((const u16*)(ws + OFF_MOD) + 2560 + n02);
        SB();
#pragma unroll
        for (int i = 0; i < 11; ++i) {
            if (i + 3 < 11) K4_LOADH(i + 3);
            if (i < 10 || kq < 3) {
#pragma unroll
                for (int nf = 0; nf < 4; ++nf)
#pragma unroll
                    for (int mf = 0; mf < 2; ++mf) acc[nf][mf] = MFMA32(wdf[i][nf], hf[i][mf], acc[nf][mf]);
            }
            SB();
        }
        f32x4* sR = (f32x4*)smem;
#pragma unroll
        for (int nf = 0; nf < 4; ++nf)
#pragma unroll
            for (int mf = 0; mf < 2; ++mf) sR[(wv * 8 + nf * 2 + mf) * 64 + l] = acc[nf][mf];
        __syncthreads();
        f32x4 a0 = zero, a1 = zero;
#pragma unroll
        for (int ww = 0; ww < 4; ++ww) {
            a0 += sR[((mh * 4 + ww) * 8 + (2 * fp) * 2 + (kq >> 1)) * 64 + l];
            a1 += sR[((mh * 4 + ww) * 8 + (2 * fp + 1) * 2 + (kq >> 1)) * 64 + l];
        }
        u32x4 o;
        o[0] = pk_bf16(bf_lo(xv[0]) + bf_lo(gv[0]) * a0[0], bf_hi(xv[0]) + bf_hi(gv[0]) * a0[1]);
        o[1] = pk_bf16(bf_lo(xv[1]) + bf_lo(gv[1]) * a0[2], bf_hi(xv[1]) + bf_hi(gv[1]) * a0[3]);
        o[2] = pk_bf16(bf_lo(xv[2]) + bf_lo(gv[2]) * a1[0], bf_hi(xv[2]) + bf_hi(gv[2]) * a1[1]);
        o[3] = pk_bf16(bf_lo(xv[3]) + bf_lo(gv[3]) * a1[2], bf_hi(xv[3]) + bf_hi(gv[3]) * a1[3]);
        *(u32x4*)(out + (size_t)tok2 * D_ + n02) = o;
    }
    TSW_FLUSH(2);
    TS_FLUSH(1);
}

extern "C" void kernel_launch(void* const* d_in, const int* in_sizes, int n_in,
                              void* d_out, int out_size, void* d_ws, size_t ws_size,
                              hipStream_t stream) {
    const u16* x = (const u16*)d_in[0];
    const u16* wqkv = (const u16*)d_in[1];
    const u16* wphi = (const u16*)d_in[2];
    const u16* wo = (const u16*)d_in[3];
    const u16* wg = (const u16*)d_in[5];
    const u16* wu = (const u16*)d_in[6];
    const u16* wd = (const u16*)d_in[7];
    const u16* cvec = (const u16*)d_in[8];
    const u16* wmod = (const u16*)d_in[9];
    char* ws = (char*)d_ws;
    k0_prep<<<K0_GRID, 256, 0, stream>>>(x, wqkv, wphi, cvec, wmod, wo, ws);
    kf_main<<<256, 512, 0, stream>>>(x, wg, wu, wd, ws, (u16*)d_out);
}
```

```cpp
#include <hip/hip_runtime.h>
#include <hip/hip_bf16.h>
#include <stdint.h>

typedef short bf16x8 __attribute__((ext_vector_type(8)));
typedef short bf16x4 __attribute__((ext_vector_type(4)));
typedef float f32x4 __attribute__((ext_vector_type(4)));
typedef float f32x2 __attribute__((ext_vector_type(2)));
typedef unsigned u32x4 __attribute__((ext_vector_type(4)));
typedef unsigned u32x2 __attribute__((ext_vector_type(2)));
typedef __bf16 bf16x2_t __attribute__((ext_vector_type(2)));
typedef unsigned short u16;

#define PT_ST 2
#define K3_LA 4
#define DI __device__ __forceinline__
#define MFMA32(a, b, c) __builtin_amdgcn_mfma_f32_16x16x32_bf16(__builtin_bit_cast(bf16x8, (a)), __builtin_bit_cast(bf16x8, (b)), (c), 0, 0, 0)
#define SB() __builtin_amdgcn_sched_barrier(0)

constexpr int S_ = 2048, D_ = 512, H_ = 32, F_ = 1376;
constexpr int NMF = S_ / 16;
constexpr int KS_D = D_ / 32;
constexpr int KS_F = F_ / 32;
constexpr int NPAIR = F_ / 16;
constexpr float LOG2E = 1.4426950408889634f;

constexpr size_t OFF_MOD  = 0;
constexpr size_t OFF_CNT  = 8192;
constexpr size_t OFF_KVF  = OFF_CNT + 49152;
constexpr size_t OFF_ZF   = OFF_KVF + 65536;
constexpr size_t OFF_SLAB = OFF_ZF + 4096;
constexpr size_t OFF_ZSL  = OFF_SLAB + (size_t)32 * 32 * 1024;
constexpr size_t OFF_XN   = OFF_ZSL + (size_t)32 * 32 * 128;
constexpr size_t OFF_WQKV = OFF_XN + (size_t)S_ * D_ * 2;
constexpr size_t OFF_WO   = OFF_WQKV + (size_t)96 * 16384;
constexpr size_t OFF_WGU  = OFF_WO + (size_t)32 * 16384;
constexpr size_t OFF_WD   = OFF_WGU + (size_t)88 * 2 * 16384;
constexpr size_t OFF_PHIQ = OFF_WD + (size_t)32 * KS_F * 1024;
constexpr size_t OFF_X1   = OFF_PHIQ + (size_t)NMF * 32 * 1024;
constexpr size_t OFF_ST   = OFF_X1 + (size_t)S_ * D_ * 2;
constexpr size_t OFF_C12  = OFF_ST + (size_t)S_ * 16 * 8;
constexpr size_t OFF_WGU2 = OFF_C12 + (size_t)88 * 1024;
constexpr size_t OFF_HH   = OFF_WGU2 + (size_t)88 * 2 * 16384;

#define TS_DECL
#define TS(idx)
#define TS_FLUSH(kid)
#define TSW(idx)
#define TSW_FLUSH(kid)

DI unsigned pk_bf16(float a, float b) {
    f32x2 v = {a, b};
    bf16x2_t r = __builtin_convertvector(v, bf16x2_t);
    return __builtin_bit_cast(unsigned, r);
}
DI float bf_lo(unsigned u) { return __uint_as_float(u << 16); }
DI float bf_hi(unsigned u) { return __uint_as_float(u & 0xffff0000u); }
DI float bf_round(float a) { return bf_lo(pk_bf16(a, 0.f)); }
DI float fexp2(float x) { return __builtin_amdgcn_exp2f(x); }
DI float frcp(float x) { return __builtin_amdgcn_rcpf(x); }

template <int KMAP, int NMAP>
DI void pretile_wave(const u16* __restrict__ W, int ldw, int ncols, int ks, int cgrp, u32x4* __restrict__ dst, int nks, int nf_mul, int nf_add,
                     u32x4* sm  ) {
    const int l = threadIdx.x & 63, g = l >> 4, c8 = l & 15;
    const int col0 = cgrp * 128 + c8 * 8;
    const bool ok = col0 < ncols;
    const int colc = ok ? col0 : 0;
    u32x4 r[8];
#pragma unroll
    for (int j = 0; j < 8; ++j) {
        const int k = ks * 32 + (KMAP == 0 ? 8 * g + j : 16 * (j >> 2) + 4 * g + (j & 3));
        r[j] = *(const u32x4*)(W + (size_t)k * ldw + colc);
    }
#pragma unroll
    for (int c = 0; c < 8; ++c) {
        u32x4 o;
#pragma unroll
        for (int jj = 0; jj < 4; ++jj) {
            const unsigned a = r[2 * jj][c >> 1], b = r[2 * jj + 1][c >> 1];
            o[jj] = (c & 1) ? ((a >> 16) | (b & 0xffff0000u)) : ((a & 0xffffu) | (b << 16));
        }
        const int n = c8 * 8 + c;
        int nf, rr;
        if (NMAP == 0) { nf = n >> 4; rr = n & 15; }
        else { const int cc = n & 31; nf = 2 * (n >> 5) + ((cc >> 2) & 1); rr = 4 * (cc >> 3) + (cc & 3); }
        sm[nf * 65 + g * 16 + rr] = o;
    }
    asm volatile("s_waitcnt lgkmcnt(0)" ::: "memory");
    __builtin_amdgcn_wave_barrier();
#pragma unroll
    for (int nf = 0; nf < 8; ++nf) {
        const int nfg = cgrp * 8 + nf;
        if (nfg * 16 < ncols) {
            const u32x4 o = sm[nf * 65 + l];
            u32x4* p_ = &dst[((size_t)(nfg * nf_mul + nf_add) * nks + ks) * 64 + l];
            asm volatile("global_store_dwordx4 %0, %1, off sc1\n\ts_nop 1" :: "v"(p_), "v"(o) : "memory");
        }
    }
}

DI float u2f(unsigned u) { return __uint_as_float(u); }
DI unsigned f2u(float f) { return __float_as_uint(f); }
DI void red2_max(float a, float b, float& A, float& B) {
    auto r = __builtin_amdgcn_permlane32_swap(f2u(a), f2u(b), false, false);
    const float c = fmaxf(u2f(r[0]), u2f(r[1]));
    auto r2 = __builtin_amdgcn_permlane16_swap(f2u(c), f2u(c), false, false);
    const float d = fmaxf(u2f(r2[0]), u2f(r2[1]));
    auto r3 = __builtin_amdgcn_permlane32_swap(f2u(d), f2u(d), false, false);
    A = u2f(r3[0]); B = u2f(r3[1]);
}
DI void red2_add(float a, float b, float& A, float& B) {
    auto r = __builtin_amdgcn_permlane32_swap(f2u(a), f2u(b), false, false);
    const float c = u2f(r[0]) + u2f(r[1]);
    auto r2 = __builtin_amdgcn_permlane16_swap(f2u(c), f2u(c), false, false);
    const float d = u2f(r2[0]) + u2f(r2[1]);
    auto r3 = __builtin_amdgcn_permlane32_swap(f2u(d), f2u(d), false, false);
    A = u2f(r3[0]); B = u2f(r3[1]);
}

constexpr int K0_MOD = 96, K0_XN = 128, K0_FOLD = 64, K0_WV = 16, K0_WO = 16, K0_WG = 0, K0_WU = 0, K0_WD = 0;
constexpr int K0_B0 = K0_MOD, K0_B1 = K0_B0 + K0_XN, K0_B2 = K0_B1 + K0_FOLD, K0_B3 = K0_B2 + K0_WV;
constexpr int K0_B4 = K0_B3 + K0_WO, K0_B5 = K0_B4 + K0_WG, K0_B6 = K0_B5 + K0_WU, K0_B7 = K0_B6 + K0_WD;
constexpr int K0_GRID = K0_B7;

__global__ __launch_bounds__(256) void k0_prep(const u16* __restrict__ x, const u16* __restrict__ wqkv, const u16* __restrict__ wphi,
                                               const u16* __restrict__ cvec, const u16* __restrict__ wmod,
                                               const u16* __restrict__ wo, char* __restrict__ ws) {
    __shared__ __attribute__((aligned(16))) u32x4 smT[4 * 520];
    float* red = (float*)smT;
    TS_DECL; TS(0);
    const int pb = blockIdx.x, t = threadIdx.x;
    const int b = pb < 160 ? pb + 96 : (pb < 256 ? pb - 160 : pb);
    if (b >= K0_B0 && b < K0_B0 + 12) ((u32x4*)(ws + OFF_CNT))[(b - K0_B0) * 256 + t] = (u32x4){0u, 0u, 0u, 0u};
    if (b < K0_B0) {
        const int cb = (b & 7) * 12 + (b >> 3);
        const int cgp = t & 3, kg = t >> 2;
        float acc[8];
#pragma unroll
        for (int i = 0; i < 8; ++i) acc[i] = 0.f;
        u32x4 wv[8]; float sc[8];
        const unsigned c2 = ((const unsigned*)cvec)[t];
#pragma unroll
        for (int i = 0; i < 8; ++i) wv[i] = *(const u32x4*)(wmod + (size_t)(kg + 64 * i) * 3072 + cb * 32 + cgp * 8);
        SB();
        {
            const float c0 = bf_lo(c2), c1 = bf_hi(c2);
            *(f32x2*)(red + 256 + 2 * t) = (f32x2){bf_round(c0 / (1.f + expf(-c0))), bf_round(c1 / (1.f + expf(-c1)))};
        }
        __syncthreads();
#pragma unroll
        for (int i = 0; i < 8; ++i) sc[i] = red[256 + kg + 64 * i];
#pragma unroll
        for (int i = 0; i < 8; ++i) {
#pragma unroll
            for (int j = 0; j < 4; ++j) {
                acc[2 * j] += sc[i] * bf_lo(wv[i][j]);
                acc[2 * j + 1] += sc[i] * bf_hi(wv[i][j]);
            }
        }
#pragma unroll
        for (int j = 0; j < 8; ++j) {
            acc[j] += u2f(__builtin_amdgcn_update_dpp(0, f2u(acc[j]), 0x124, 0xf, 0xf, false));
            acc[j] += u2f(__builtin_amdgcn_update_dpp(0, f2u(acc[j]), 0x128, 0xf, 0xf, false));
        }
#pragma unroll
        for (int j = 0; j < 8; j += 2) red2_add(acc[j], acc[j + 1], acc[j], acc[j + 1]);
        if ((t & 63) < 4) {
#pragma unroll
            for (int j = 0; j < 8; ++j) red[(t >> 6) * 32 + cgp * 8 + j] = acc[j];
        }
        __syncthreads();
        if (t < 32) {
            float s = bf_round((red[t] + red[32 + t]) + (red[64 + t] + red[96 + t]));
            const int n = cb * 32 + t, row = n >> 9;
            if (row == 1 || row == 4) s = 1.f + s;
            ((u16*)(ws + OFF_MOD))[n] = (u16)(pk_bf16(s, 0.f) & 0xffffu);
        }
    } else if (b < K0_B1) {
        const int mf = b - K0_B0, r = t & 15, c = t >> 4;
        const u16* xr = x + (size_t)(mf * 16 + r) * D_;
        u32x4 v[4];
#pragma unroll
        for (int i = 0; i < 4; ++i) v[i] = *(const u32x4*)(xr + (c + 16 * i) * 8);
        float f[32];
        float s = 0.f;
#pragma unroll
        for (int i = 0; i < 4; ++i)
#pragma unroll
            for (int j = 0; j < 4; ++j) { f[i * 8 + 2 * j] = bf_lo(v[i][j]); f[i * 8 + 2 * j + 1] = bf_hi(v[i][j]); s += f[i * 8 + 2 * j] + f[i * 8 + 2 * j + 1]; }
        s += __shfl_xor(s, 16); s += __shfl_xor(s, 32);
        if ((t & 63) < 16) red[(t >> 6) * 16 + r] = s;
        __syncthreads();
        const float m = ((red[r] + red[16 + r]) + (red[32 + r] + red[48 + r])) * (1.f / D_);
        float q = 0.f;
#pragma unroll
        for (int i = 0; i < 32; ++i) { f[i] -= m; q += f[i] * f[i]; }
        q += __shfl_xor(q, 16); q += __shfl_xor(q, 32);
        if ((t & 63) < 16) red[64 + (t >> 6) * 16 + r] = q;
        __syncthreads();
        const float var = (red[64 + r] + red[80 + r]) + (red[96 + r] + red[112 + r]);
        const float rs = rsqrtf(var * (1.f / D_) + 1e-5f);
        u32x4* dst = (u32x4*)(ws + OFF_XN) + (size_t)mf * 16 * 64;
#pragma unroll
        for (int i = 0; i < 4; ++i) {
            u32x4 o;
#pragma unroll
            for (int j = 0; j < 4; ++j) o[j] = pk_bf16(f[i * 8 + 2 * j] * rs, f[i * 8 + 2 * j + 1] * rs);
            const int ch = c + 16 * i;
            dst[(ch >> 2) * 64 + (ch & 3) * 16 + r] = o;
        }
    } else if (b < K0_B2) {
        const int wv = (b - K0_B1) * 4 + (t >> 6);
        const int hq = wv >> 2, kq = wv & 3;
        const int l = t & 63, r = l & 15, g = l >> 4, h = hq & 31;
        bf16x4 bphi;
#pragma unroll
        for (int j = 0; j < 4; ++j) bphi[j] = (short)wphi[h * 256 + (4 * g + j) * 16 + r];
        char* dst = ws + OFF_WQKV + (size_t)hq * 16384;
        bf16x4 a[8];
#pragma unroll
        for (int i = 0; i < 8; ++i) a[i] = *(const bf16x4*)(wqkv + (size_t)((kq * 8 + i) * 16 + r) * 1536 + hq * 16 + 4 * g);
#pragma unroll
        for (int i = 0; i < 8; ++i) {
            const int rb = kq * 8 + i;
            f32x4 d = {0.f, 0.f, 0.f, 0.f};
            d = __builtin_amdgcn_mfma_f32_16x16x16bf16_1k(a[i], bphi, d, 0, 0, 0);
            u32x2 o;
            o[0] = pk_bf16(d[0] * LOG2E, d[1] * LOG2E);
            o[1] = pk_bf16(d[2] * LOG2E, d[3] * LOG2E);
            const int ks = rb >> 1, gp = 2 * (rb & 1) + (g >> 1);
            *(u32x2*)(dst + ks * 1024 + (gp * 16 + r) * 16 + (g & 1) * 8) = o;
        }
    } else {
        u32x4* sm = smT + (t >> 6) * 520;
        if (b < K0_B3) {
            const int id = (b - K0_B2) * 4 + (t >> 6);
            pretile_wave<0, 0>(wqkv + 1024, 1536, 512, id >> 2, id & 3, (u32x4*)(ws + OFF_WQKV), KS_D, 1, 64, sm);
        } else {
            const int id = (b - K0_B3) * 4 + (t >> 6);
            pretile_wave<1, 1>(wo, 512, 512, id >> 2, id & 3, (u32x4*)(ws + OFF_WO), KS_D, 1, 0, sm);
        }
    }
    TS_FLUSH(0);
}

DI float fmax2(float a, float b) { return __builtin_amdgcn_fmed3f(a, b, __builtin_inff()); }
DI float fmin2(float a, float b) { return __builtin_amdgcn_fmed3f(a, b, -__builtin_inff()); }
template <int N>
DI void softmax_pm(const f32x4 (&p)[N], u32x4 (&o)[N]) {
    float a[N], b[N];
#pragma unroll
    for (int i = 0; i < N; ++i) {
        a[i] = fmax2(fmax2(p[i][0], p[i][1]), fmax2(p[i][2], p[i][3]));
        b[i] = -fmin2(fmin2(p[i][0], p[i][1]), fmin2(p[i][2], p[i][3]));
    }
#pragma unroll
    for (int i = 0; i < N; ++i) { auto r = __builtin_amdgcn_permlane32_swap(f2u(a[i]), f2u(b[i]), false, false); a[i] = fmax2(u2f(r[0]), u2f(r[1])); }
#pragma unroll
    for (int i = 0; i < N; ++i) { auto r = __builtin_amdgcn_permlane16_swap(f2u(a[i]), f2u(a[i]), false, false); a[i] = fmax2(u2f(r[0]), u2f(r[1])); }
#pragma unroll
    for (int i = 0; i < N; ++i) { auto r = __builtin_amdgcn_permlane32_swap(f2u(a[i]), f2u(a[i]), false, false); a[i] = u2f(r[0]); b[i] = u2f(r[1]); }
    float ep[N][4], en[N][4];
#pragma unroll
    for (int i = 0; i < N; ++i)
#pragma unroll
        for (int j = 0; j < 4; ++j) { ep[i][j] = fexp2(p[i][j] - a[i]); en[i][j] = fexp2(-b[i] - p[i][j]); }
#pragma unroll
    for (int i = 0; i < N; ++i) { a[i] = (ep[i][0] + ep[i][1]) + (ep[i][2] + ep[i][3]); b[i] = (en[i][0] + en[i][1]) + (en[i][2] + en[i][3]); }
#pragma unroll
    for (int i = 0; i < N; ++i) { auto r = __builtin_amdgcn_permlane32_swap(f2u(a[i]), f2u(b[i]), false, false); a[i] = u2f(r[0]) + u2f(r[1]); }
#pragma unroll
    for (int i = 0; i < N; ++i) { auto r = __builtin_amdgcn_permlane16_swap(f2u(a[i]), f2u(a[i]), false, false); a[i] = frcp(u2f(r[0]) + u2f(r[1])); }
#pragma unroll
    for (int i = 0; i < N; ++i) { auto r = __builtin_amdgcn_permlane32_swap(f2u(a[i]), f2u(a[i]), false, false); a[i] = u2f(r[0]); b[i] = u2f(r[1]); }
#pragma unroll
    for (int i = 0; i < N; ++i) {
        o[i][0] = pk_bf16(ep[i][0] * a[i], ep[i][1] * a[i]); o[i][1] = pk_bf16(ep[i][2] * a[i], ep[i][3] * a[i]);
        o[i][2] = pk_bf16(en[i][0] * b[i], en[i][1] * b[i]); o[i][3] = pk_bf16(en[i][2] * b[i], en[i][3] * b[i]);
    }
}

DI u32x4 modulate8(const u32x4 xv, const float (&gg)[8], const float (&ss)[8]) {
    u32x4 o;
#pragma unroll
    for (int j = 0; j < 4; ++j)
        o[j] = pk_bf16(bf_lo(xv[j]) * gg[2 * j] + ss[2 * j], bf_hi(xv[j]) * gg[2 * j + 1] + ss[2 * j + 1]);
    return o;
}

#define TSD(i)
#define TSA(i) TS(i)
#define TSB(i)
#define TSC(i)
#define RLX_AGENT __ATOMIC_RELAXED, __HIP_MEMORY_SCOPE_AGENT
constexpr int CNT_CK = 0, CNT_CV = 32, CNT_CP = 64, CNT_CX = 96, CNT_CH = 128, CNT_CW = 150, CNT_CT = 170, CNT_CD = 172;
typedef __amdgpu_buffer_rsrc_t rsrc_t;
DI u32x4 bld(rsrc_t rs, unsigned off) { return __builtin_amdgcn_raw_buffer_load_b128(rs, off, 0, 16); }
DI u32x2 bld64(rsrc_t rs, unsigned off) { return __builtin_amdgcn_raw_buffer_load_b64(rs, off, 0, 16); }
DI void bst(rsrc_t rs, unsigned off, u32x4 v) { __builtin_amdgcn_raw_buffer_store_b128(v, rs, off, 0, 16); }
DI void bst64(rsrc_t rs, unsigned off, u32x2 v) { __builtin_amdgcn_raw_buffer_store_b64(v, rs, off, 0, 16); }
#define POLL_SLEEP 4
#define DRAIN() asm volatile("s_waitcnt vmcnt(0)" ::: "memory")
DI void wait_cnt(unsigned* c, unsigned n) {
    unsigned sp = 0;
    const unsigned lane = threadIdx.x & 63;
    unsigned* p = c + (lane < n ? lane : 0);
    while (!__all(__hip_atomic_load(p, RLX_AGENT) != 0u)) { __builtin_amdgcn_s_sleep(POLL_SLEEP); if (++sp > (1u << 18)) break; }
    __builtin_amdgcn_fence(__ATOMIC_ACQUIRE, "wavefront");
    asm volatile("" ::: "memory");
}
DI void set_flag(unsigned* c) { __hip_atomic_store(c, 1u, RLX_AGENT); }
DI float gelu_tanh(float g) {
    const float k0 = -2.f * 0.7978845608028654f * LOG2E, k1 = k0 * 0.044715f;
    const float a = g * (g * g * k1 + k0);
    return g * frcp(1.f + fexp2(a));
}
DI u32x4 scale8(const u32x4 v, const u32x4 gv) {
    u32x4 o;
#pragma unroll
    for (int j = 0; j < 4; ++j) o[j] = pk_bf16(bf_lo(v[j]) * bf_lo(gv[j]), bf_hi(v[j]) * bf_hi(gv[j]));
    return o;
}

#define B3_EARLY 10
#define B3_K 12
#define B3_A 16
#define B3_PER 0
#define B3_MID 12
extern "C" __global__ __launch_bounds__(512, 2) void kf_main(const u16* __restrict__ x, const u16* __restrict__ wg, const u16* __restrict__ wu, const u16* __restrict__ wd,
                                                                char* __restrict__ ws, u16* __restrict__ out) {
    __shared__ __attribute__((aligned(16))) u32x4 smem[9216 + 128 + 128 + 64 + 128 + 4];
    TS_DECL; TS(0);
    const int xcd = blockIdx.x & 7, bi = blockIdx.x >> 3;
    const int wv = __builtin_amdgcn_readfirstlane(threadIdx.x >> 6);
    const int l = threadIdx.x & 63, g = l >> 4;
    unsigned* cnt = (unsigned*)(ws + OFF_CNT);
    const rsrc_t rs = __builtin_amdgcn_make_buffer_rsrc(ws, 0, 0x4000000, 0x00020000);
    u32x4* sWO = smem + 5120;
    u32x4* sZ = smem + 9216;
    u32x4* sGB = smem + 9216 + 128;
    f32x2* sSt = (f32x2*)(smem + 9216 + 256);
    volatile unsigned* sFlag = (volatile unsigned*)(smem + 9216 + 448);
    const u32x4 ones = {0x3f803f80u, 0x3f803f80u, 0x3f803f80u, 0x3f803f80u};
    const f32x4 zero = {0.f, 0.f, 0.f, 0.f};

    const int mt2 = 4 * xcd + (bi >> 3), nt2 = bi & 7;
    const int kq = wv & 3, mh = wv >> 2;
    const int t0_ = (242 * xcd) >> 3, t1_ = (242 * (xcd + 1)) >> 3;
    const int id3 = t0_ + bi;
    const bool act3 = id3 < t1_;
    const int id3c = act3 ? id3 : t0_;
    const int ng = (id3c % 121) / 11, mt3 = 11 * (id3c / 121) + (id3c % 121) % 11;
    const int mf0 = mt3 * 6;
    const int nmf = (NMF - mf0) < 6 ? (NMF - mf0) : 6;
    const int pair = ng * 8 + wv;
    const bool actw = pair < NPAIR;
    const unsigned wgoff = (unsigned)(OFF_WGU2 + ((size_t)(actw ? pair : ng * 8) * 2048 + l) * 16);
    u32x4 bg[16], bu[16];
#define K3_LOADB(ks) do { bg[ks] = bld(rs, wgoff + (ks) * 1024); bu[ks] = bld(rs, wgoff + 16384 + (ks) * 1024); SB(); } while (0)

#define WO_DMA(part) do { const u32x4* WO_ = (const u32x4*)(ws + OFF_WO) + ((size_t)nt2 * 64 + (part) * 8) * 64 + l; \
        const unsigned ldsb_ = (unsigned)(size_t)(sWO + (part) * 8 * 64); \
        _Pragma("unroll") for (int i_ = 0; i_ < 8; ++i_) { unsigned keep_; \
            asm volatile("s_mov_b32 %0, m0\n\ts_mov_b32 m0, %2\n\ts_nop 0\n\tglobal_load_lds_dwordx4 %1, off\n\ts_mov_b32 m0, %0" : "=&s"(keep_) : "v"(WO_ + i_ * 64), "s"(ldsb_ + i_ * 1024) : "memory"); } } while (0)
    if (threadIdx.x < 16) sFlag[threadIdx.x] = 0u;
    {
        u32x4* sA = smem;
        char* sP = (char*)(smem + 4096);
        const int mt = 8 * (xcd >> 1) + (bi >> 2), hg = 4 * (xcd & 1) + (bi & 3);
        const int w = wv & 3;
        const int h = hg * 4 + w;
        const u32x4* WQ = (const u32x4*)(ws + OFF_WQKV) + (size_t)h * 1024 + l;
        u32x4 af[2][4];
#define K1_RDA(buf, ks) do { _Pragma("unroll") for (int mf = 0; mf < 4; ++mf) af[buf][mf] = sA[(mf * 16 + (ks)) * 64 + l]; } while (0)
        const u32x4* XN = (const u32x4*)(ws + OFF_XN) + (size_t)mt * 4096 + mh * 2048 + w * 64 + l;
        const u32x4* MS = (const u32x4*)(ws + OFF_MOD) + w * 4 + g;
        const u32x4* WB = WQ + (wv < 4 ? 32 : 64) * 1024;
        u32x4 xa[4][2], gs[4][2], bb[16];
#define K1_LOADA(c) do { gs[c][0] = MS[64 + (c) * 16]; gs[c][1] = MS[(c) * 16]; \
        _Pragma("unroll") for (int mf = 0; mf < 2; ++mf) xa[c][mf] = XN[mf * 1024 + (c) * 256]; SB(); } while (0)
#define K1_PREP1(c, mf) do { float gg[8], ss[8]; \
        _Pragma("unroll") for (int j = 0; j < 4; ++j) { gg[2 * j] = bf_lo(gs[c][0][j]); gg[2 * j + 1] = bf_hi(gs[c][0][j]); ss[2 * j] = bf_lo(gs[c][1][j]); ss[2 * j + 1] = bf_hi(gs[c][1][j]); } \
        sA[((2 * mh + (mf)) * 16 + 4 * (c) + w) * 64 + l] = modulate8(xa[c][mf], gg, ss); } while (0)
#define K1_LOADB(ks) do { bb[ks] = WB[(ks) * 64]; } while (0)
        f32x4 ac[4];
#pragma unroll
        for (int mf = 0; mf < 4; ++mf) ac[mf] = zero;
        K1_LOADA(0); K1_LOADA(1);
        K1_LOADB(0); K1_LOADB(1); K1_LOADB(2); K1_LOADB(3); SB();
        K1_LOADA(2); K1_LOADA(3);
        K1_LOADB(4); K1_LOADB(5); K1_LOADB(6); K1_LOADB(7); SB();
        K1_PREP1(0, 0); K1_PREP1(0, 1); K1_PREP1(1, 0); K1_PREP1(1, 1);
        __syncthreads(); SB();
        TSC(5);
        u32x4 bq[16];
        u32x4 a3[3][4];
#define K1_RD3(ks) do { _Pragma("unroll") for (int mf = 0; mf < 4; ++mf) a3[(ks) % 3][mf] = sA[(mf * 16 + (ks)) * 64 + l]; } while (0)
        K1_RD3(0); K1_RD3(1);
#pragma unroll
        for (int ks = 0; ks < 16; ++ks) {
            if (ks + 8 < 16) K1_LOADB(ks + 8);
            else if (wv >= 4) bq[ks - 8] = WQ[(ks - 8) * 64];
            if (ks == 6) { __syncthreads(); }
            if (ks + 2 < 16) K1_RD3(ks + 2);
#pragma unroll
            for (int mf = 0; mf < 4; ++mf) ac[mf] = MFMA32(bb[ks], a3[ks % 3][mf], ac[mf]);
            if (ks == 0) K1_PREP1(2, 0);
            if (ks == 1) K1_PREP1(2, 1);
            if (ks == 2) K1_PREP1(3, 0);
            if (ks == 3) K1_PREP1(3, 1);
            SB();
        }
        char* sV = (char*)(sWO + 3584);
        if (wv < 4) {
            __builtin_amdgcn_s_setprio(3);
            TSC(1);
            char* myP = sP + w * 4096;
            {
                u32x4 o[4];
                softmax_pm<4>(ac, o);
#pragma unroll
                for (int mf = 0; mf < 4; ++mf) {
                    char* row = myP + (mf * 16 + (l & 15)) * 64;
                    *(u32x2*)(row + g * 8) = (u32x2){o[mf][0], o[mf][1]};
                    *(u32x2*)(row + 32 + g * 8) = (u32x2){o[mf][2], o[mf][3]};
                }
            }
            TSC(2);
            const unsigned base = (unsigned)(size_t)myP + (4 * g + ((l & 15) >> 2)) * 64 + (l & 3) * 8;
            u32x2 t0, t1, t2, t3, t4, t5, t6, t7;
            asm volatile("s_waitcnt lgkmcnt(0)\n\t"
                         "ds_read_b64_tr_b16 %0, %8\n\tds_read_b64_tr_b16 %1, %8 offset:1024\n\t"
                         "ds_read_b64_tr_b16 %2, %8 offset:32\n\tds_read_b64_tr_b16 %3, %8 offset:1056\n\t"
                         "ds_read_b64_tr_b16 %4, %8 offset:2048\n\tds_read_b64_tr_b16 %5, %8 offset:3072\n\t"
                         "ds_read_b64_tr_b16 %6, %8 offset:2080\n\tds_read_b64_tr_b16 %7, %8 offset:3104\n\t"
                         "s_waitcnt lgkmcnt(0)"
                         : "=&v"(t0), "=&v"(t1), "=&v"(t2), "=&v"(t3), "=&v"(t4), "=&v"(t5), "=&v"(t6), "=&v"(t7) : "v"(base) : "memory");
            if (l == 0) __hip_atomic_fetch_add((unsigned*)&sFlag[2], 16u, __ATOMIC_RELAXED, __HIP_MEMORY_SCOPE_WORKGROUP);
            const u32x4 pa00 = {t0[0], t0[1], t1[0], t1[1]}, pa01 = {t2[0], t2[1], t3[0], t3[1]};
            const u32x4 pa10 = {t4[0], t4[1], t5[0], t5[1]}, pa11 = {t6[0], t6[1], t7[0], t7[1]};
            { unsigned sp = 0; while (sFlag[8 + w] == 0u) { __builtin_amdgcn_s_sleep(1); if (++sp > (1u << 22)) break; } }
            asm volatile("" ::: "memory");
            u32x4 vb0, vb1;
            {
                const unsigned vbase = (unsigned)(size_t)sV + w * 2048 + (4 * g + ((l & 15) >> 2)) * 32 + (l & 3) * 8;
                u32x2 v0, v1, v2, v3;
                asm volatile("ds_read_b64_tr_b16 %0, %4\n\tds_read_b64_tr_b16 %1, %4 offset:512\n\t"
                             "ds_read_b64_tr_b16 %2, %4 offset:1024\n\tds_read_b64_tr_b16 %3, %4 offset:1536\n\ts_waitcnt lgkmcnt(0)"
                             : "=&v"(v0), "=&v"(v1), "=&v"(v2), "=&v"(v3) : "v"(vbase) : "memory");
                vb0 = (u32x4){v0[0], v0[1], v1[0], v1[1]}; vb1 = (u32x4){v2[0], v2[1], v3[0], v3[1]};
            }
            f32x4 kv0 = MFMA32(pa00, vb0, zero), kv1 = MFMA32(pa01, vb0, zero);
            f32x4 z0 = MFMA32(pa00, ones, zero), z1 = MFMA32(pa01, ones, zero);
            kv0 = MFMA32(pa10, vb1, kv0); kv1 = MFMA32(pa11, vb1, kv1);
            z0 = MFMA32(pa10, ones, z0); z1 = MFMA32(pa11, ones, z1);
            u32x4 o;
            o[0] = pk_bf16(kv0[0], kv0[1]); o[1] = pk_bf16(kv0[2], kv0[3]); o[2] = pk_bf16(kv1[0], kv1[1]); o[3] = pk_bf16(kv1[2], kv1[3]);
            bst(rs, (unsigned)(OFF_SLAB + (((size_t)h * 32 + mt) * 64 + l) * 16), o);
            if ((l & 15) < 2) {
                const f32x4 zz = (l & 1) ? z1 : z0;
                bst(rs, (unsigned)(OFF_ZSL + (((size_t)h * 32 + mt) * 8 + g * 2 + (l & 1)) * 16), __builtin_bit_cast(u32x4, zz));
            }
            TSC(3);
            DRAIN();
            TSC(4);
            if (l == 0) set_flag(cnt + (CNT_CK + h) * 64 + mt);
            TSA(1);
            __builtin_amdgcn_s_setprio(0);
            if (l == 0) __hip_atomic_fetch_add((unsigned*)&sFlag[5], 1u, __ATOMIC_RELAXED, __HIP_MEMORY_SCOPE_WORKGROUP);
            const bool dunit = wv == 0 && blockIdx.x < 172;
            const bool wunit = (wv == 1 || wv == 2) && blockIdx.x >= 84;
            const int wmat = wv - 1, wcg = ((int)blockIdx.x - 84) >> 2, wkq = blockIdx.x & 3;
            u32x4 rr[1][8], wgam[4], wbet[4];
            if (dunit) {
                const int ks = blockIdx.x >> 2, cgrp = blockIdx.x & 3;
#pragma unroll
                for (int j = 0; j < 8; ++j) rr[0][j] = *(const u32x4*)(wd + (size_t)(ks * 32 + 16 * (j >> 2) + 4 * g + (j & 3)) * 512 + cgrp * 128 + (l & 15) * 8);
            }
            if (wunit) {
                const u16* W = wmat ? wu : wg;
#pragma unroll
                for (int j = 0; j < 8; ++j)
                    rr[0][j] = *(const u32x4*)(W + (size_t)((wkq * 4 + (l >> 4)) * 32 + 8 * ((l >> 2) & 3) + j) * F_ + wcg * 32 + (l & 3) * 8);
#pragma unroll
                for (int i = 0; i < 4; ++i) {
                    wgam[i] = ((const u32x4*)(ws + OFF_MOD))[256 + (wkq * 4 + i) * 4 + g];
                    wbet[i] = ((const u32x4*)(ws + OFF_MOD))[192 + (wkq * 4 + i) * 4 + g];
                }
            }
            SB();
            if (wv == 3) {
                { unsigned sp = 0; while (sFlag[5] != 4u) { __builtin_amdgcn_s_sleep(1); if (++sp > (1u << 22)) break; } }
                WO_DMA(7); WO_DMA(6); WO_DMA(5); WO_DMA(4);
            }
            if (dunit || wunit) {
                { unsigned sp = 0; while (sFlag[6] != 4u || (sFlag[2] >> 4) != 4u) { __builtin_amdgcn_s_sleep(1); if (++sp > (1u << 22)) break; } }
                asm volatile("" ::: "memory");
            }
            TSD(2);
            TSD(3);
            u32x4* sm = smem + w * 1040;
            if (dunit) {
                const int ks = blockIdx.x >> 2, cgrp = blockIdx.x & 3, c8 = l & 15;
#pragma unroll
                for (int c = 0; c < 8; ++c) {
                    u32x4 t_;
#pragma unroll
                    for (int jj = 0; jj < 4; ++jj) {
                        const unsigned a = rr[0][2 * jj][c >> 1], b = rr[0][2 * jj + 1][c >> 1];
                        t_[jj] = (c & 1) ? ((a >> 16) | (b & 0xffff0000u)) : ((a & 0xffffu) | (b << 16));
                    }
                    const int n = c8 * 8 + c, cc = n & 31;
                    sm[(2 * (n >> 5) + ((cc >> 2) & 1)) * 65 + g * 16 + 4 * (cc >> 3) + (cc & 3)] = t_;
                }
                asm volatile("s_waitcnt lgkmcnt(0)" ::: "memory");
                __builtin_amdgcn_wave_barrier();
#pragma unroll
                for (int nf = 0; nf < 8; ++nf) bst(rs, (unsigned)(OFF_WD + (((size_t)(cgrp * 8 + nf) * KS_F + ks) * 64 + l) * 16), sm[nf * 65 + l]);
                TSD(4);
                DRAIN();
                TSD(5);
                if (l == 0) set_flag(cnt + (CNT_CD + cgrp) * 64 + ks);
            }
            if (wunit) {
                const int k0 = wkq * 4;
#pragma unroll
                for (int c = 0; c < 8; ++c) {
                    u32x4 t_;
#pragma unroll
                    for (int jj = 0; jj < 4; ++jj) {
                        const unsigned a = rr[0][2 * jj][c >> 1], b = rr[0][2 * jj + 1][c >> 1];
                        t_[jj] = (c & 1) ? ((a >> 16) | (b & 0xffff0000u)) : ((a & 0xffffu) | (b << 16));
                    }
                    const int n = (l & 3) * 8 + c;
                    sm[((l >> 4) * 2 + (n >> 4)) * 65 + ((l >> 2) & 3) * 16 + (n & 15)] = t_;
                }
                asm volatile("s_waitcnt lgkmcnt(0)" ::: "memory");
                __builtin_amdgcn_wave_barrier();
                f32x4 c1[2], c2[2];
                c1[0] = zero; c1[1] = zero; c2[0] = zero; c2[1] = zero;
#pragma unroll
                for (int i = 0; i < 4; ++i)
#pragma unroll
                    for (int nf = 0; nf < 2; ++nf) {
                        const u32x4 v = sm[(i * 2 + nf) * 65 + l];
                        c2[nf] = MFMA32(v, wbet[i], c2[nf]);
                        const u32x4 o_ = scale8(v, wgam[i]);
                        c1[nf] = MFMA32(o_, ones, c1[nf]);
                        bst(rs, (unsigned)(OFF_WGU2 + (((size_t)((wcg * 2 + nf) * 2 + wmat) * 16 + k0 + i) * 64 + l) * 16), o_);
                    }
                if ((l & 15) < 4) {
                    const int nf = (l >> 1) & 1, which = l & 1;
                    const f32x4 cv = which ? (nf ? c2[1] : c2[0]) : (nf ? c1[1] : c1[0]);
                    bst(rs, (unsigned)(OFF_C12 + ((((size_t)(wcg * 2 + nf) * 4 + wkq) * 2 + wmat) * 2 + which) * 64 + g * 16), __builtin_bit_cast(u32x4, cv));
                }
                TSD(4);
                DRAIN();
                TSD(5);
                if (l == 0) set_flag(cnt + (CNT_CW + (wcg >> 2)) * 64 + ((wcg & 3) * 4 + wkq) * 2 + wmat);
            }
            if (wv == 0) { WO_DMA(0); WO_DMA(1); }
            if (wv == 1) { WO_DMA(2); }
            if (wv == 2) { WO_DMA(3); }
            DRAIN();
            if (wv == 3) {
                wait_cnt(cnt + (CNT_CW + ng) * 64, ng == 10 ? 24u : 32u); wait_cnt(cnt + (CNT_CD + (nt2 >> 1)) * 64, 43u);
                if (l == 0) sFlag[4] = 1u;
            } else { unsigned sp = 0; while (sFlag[4] == 0u) { __builtin_amdgcn_s_sleep(2); if (++sp > (1u << 22)) break; } }
            asm volatile("" ::: "memory");
            TSD(1);
#pragma unroll
            for (int i = 0; i < B3_K; ++i) K3_LOADB(i);
            TSA(3);
        } else {
            {
#pragma unroll
                for (int mf = 0; mf < 4; ++mf)
                    *(u32x2*)(sV + w * 2048 + (mf * 16 + (l & 15)) * 32 + g * 8) = (u32x2){pk_bf16(ac[mf][0], ac[mf][1]), pk_bf16(ac[mf][2], ac[mf][3])};
                asm volatile("s_waitcnt lgkmcnt(0)" ::: "memory");
                if (l == 0) sFlag[8 + w] = 1u;
            }
            SB();
#pragma unroll
            for (int i = 8; i < 16; ++i) bq[i] = WQ[i * 64];
            SB();
            TSC(1);
            f32x4 aq[4];
#pragma unroll
            for (int mf = 0; mf < 4; ++mf) aq[mf] = zero;
            K1_RD3(0); K1_RD3(1);
#pragma unroll
            for (int ks = 0; ks < 16; ++ks) {
                if (ks + 2 < 16) K1_RD3(ks + 2);
#pragma unroll
                for (int mf = 0; mf < 4; ++mf) aq[mf] = MFMA32(bq[ks], a3[ks % 3][mf], aq[mf]);
                SB();
            }
            TSC(3);
            if (l == 0) __hip_atomic_fetch_add((unsigned*)&sFlag[6], 1u, __ATOMIC_RELAXED, __HIP_MEMORY_SCOPE_WORKGROUP);
            SB();
            u32x4 o[4];
            softmax_pm<4>(aq, o);
            TSC(2);
            wait_cnt(cnt + (CNT_CK + h) * 64, 32u);
            TSD(1);
            {
                u32x4 sl[32], zl[4];
#pragma unroll
                for (int i = 0; i < 4; ++i) zl[i] = bld(rs, (unsigned)(OFF_ZSL + ((size_t)h * 256 + i * 64 + l) * 16));
#pragma unroll
                for (int i = 0; i < 32; ++i) sl[i] = bld(rs, (unsigned)(OFF_SLAB + (((size_t)h * 32 + i) * 64 + l) * 16));
                SB();
                TSD(2);
                float z[4];
#pragma unroll
                for (int j = 0; j < 4; ++j) z[j] = (u2f(zl[0][j]) + u2f(zl[1][j])) + (u2f(zl[2][j]) + u2f(zl[3][j]));
#pragma unroll
                for (int j = 0; j < 4; ++j) z[j] += __shfl_xor(z[j], 8);
                red2_add(z[0], z[1], z[0], z[1]); red2_add(z[2], z[3], z[2], z[3]);
                if (l < 8) ((u32x2*)sZ)[w * 8 + l] = (u32x2){pk_bf16(z[0], z[1]), pk_bf16(z[2], z[3])};
                float a[8];
#pragma unroll
                for (int j = 0; j < 4; ++j) { a[2 * j] = bf_lo(sl[0][j]); a[2 * j + 1] = bf_hi(sl[0][j]); }
#pragma unroll
                for (int i = 1; i < 32; ++i)
#pragma unroll
                    for (int j = 0; j < 4; ++j) { a[2 * j] += bf_lo(sl[i][j]); a[2 * j + 1] += bf_hi(sl[i][j]); }
                const u32x4 ka = (u32x4){pk_bf16(a[0], a[1]), pk_bf16(a[2], a[3]), pk_bf16(a[4], a[5]), pk_bf16(a[6], a[7])};
                asm volatile("s_waitcnt lgkmcnt(0)" ::: "memory");
                __builtin_amdgcn_wave_barrier();
                const u32x4 za = sZ[w * 4 + g];
                f32x4 num[4], den[4];
#pragma unroll
                for (int mf = 0; mf < 4; ++mf) { num[mf] = MFMA32(ka, o[mf], zero); den[mf] = MFMA32(za, o[mf], zero); }
#pragma unroll
                for (int mp = 0; mp < 2; ++mp) {
                    const float r0 = frcp(den[2 * mp][0] + 1e-6f), r1 = frcp(den[2 * mp + 1][0] + 1e-6f);
                    bst(rs, (unsigned)(OFF_PHIQ + (((size_t)(mt * 2 + mp) * 32 + h) * 64 + l) * 16),
                        (u32x4){pk_bf16(num[2 * mp][0] * r0, num[2 * mp][1] * r0), pk_bf16(num[2 * mp][2] * r0, num[2 * mp][3] * r0),
                                pk_bf16(num[2 * mp + 1][0] * r1, num[2 * mp + 1][1] * r1), pk_bf16(num[2 * mp + 1][2] * r1, num[2 * mp + 1][3] * r1)});
                }
            }
            TSC(4); TSD(3);
            DRAIN();
            TSC(5); TSD(4);
            if (l == 0) {
                const unsigned old = __hip_atomic_fetch_add((unsigned*)&sFlag[2], 1u, __ATOMIC_RELAXED, __HIP_MEMORY_SCOPE_WORKGROUP);
                if ((old & 15u) == 3u) set_flag(cnt + (CNT_CP + mt) * 64 + hg);
            }
            TSA(1);
            if (wv != 7) {
                { unsigned sp = 0; while (sFlag[4] == 0u) { __builtin_amdgcn_s_sleep(2); if (++sp > (1u << 22)) break; } }
                asm volatile("" ::: "memory");
#pragma unroll
                for (int i = 0; i < B3_K; ++i) K3_LOADB(i);
            }
        }
    }
    TSA(2);
    if (wv == 7) { wait_cnt(cnt + (CNT_CP + mt2) * 64, 8u); if (l == 0) sFlag[0] = 1u; }
    else { unsigned sp = 0; while (sFlag[0] == 0u) { __builtin_amdgcn_s_sleep(1); if (++sp > (1u << 22)) break; } }
    asm volatile("" ::: "memory");
    TSA(4);
    u32x4 of[4][2];
#pragma unroll
    for (int hp = 0; hp < 4; ++hp) {
        const unsigned o_ = (unsigned)(OFF_PHIQ + (((size_t)(mt2 * 2 + mh) * 32 + kq * 8 + 2 * hp) * 64 + l) * 16);
        of[hp][0] = bld(rs, o_); of[hp][1] = bld(rs, o_ + 1024);
    }
    SB();
    const int fmf = 2 * mh + (kq >> 1), fp = kq & 1;
    const int tok2 = (mt2 * 4 + fmf) * 16 + (l & 15);
    const int n02 = nt2 * 64 + fp * 32 + g * 8;
    const u32x4 xv2 = *(const u32x4*)(x + (size_t)tok2 * D_ + n02);
    const u32x4 gv2 = *(const u32x4*)((const u16*)(ws + OFF_MOD) + 1024 + n02);
    SB();
    TSA(5);
    __syncthreads();
    TS(6);
    {
        f32x4 acc[4][2];
#pragma unroll
        for (int a = 0; a < 4; ++a) { acc[a][0] = zero; acc[a][1] = zero; }
#pragma unroll
        for (int hp = 0; hp < 4; ++hp) {
#pragma unroll
            for (int i = hp * B3_PER; i < (hp + 1) * B3_PER; ++i) K3_LOADB(i);
            const u32x4 o0 = {of[hp][0][0], of[hp][0][1], of[hp][1][0], of[hp][1][1]}, o1 = {of[hp][0][2], of[hp][0][3], of[hp][1][2], of[hp][1][3]};
#pragma unroll
            for (int nf = 0; nf < 4; ++nf) {
                const u32x4 wf = sWO[(nf * 16 + kq * 4 + hp) * 64 + l];
                acc[nf][0] = MFMA32(wf, o0, acc[nf][0]); acc[nf][1] = MFMA32(wf, o1, acc[nf][1]);
            }
        }
        TSB(1);
        f32x4* sR = (f32x4*)smem;
#pragma unroll
        for (int nf = 0; nf < 4; ++nf)
#pragma unroll
            for (int mf = 0; mf < 2; ++mf) sR[(wv * 8 + nf * 2 + mf) * 64 + l] = acc[nf][mf];
        __syncthreads();
        TSB(2);
        f32x4 a0 = zero, a1 = zero;
#pragma unroll
        for (int ww = 0; ww < 4; ++ww) {
            a0 += sR[((mh * 4 + ww) * 8 + (2 * fp) * 2 + (kq >> 1)) * 64 + l];
            a1 += sR[((mh * 4 + ww) * 8 + (2 * fp + 1) * 2 + (kq >> 1)) * 64 + l];
        }
        u32x4 o;
        o[0] = pk_bf16(bf_lo(xv2[0]) + bf_lo(gv2[0]) * a0[0], bf_hi(xv2[0]) + bf_hi(gv2[0]) * a0[1]);
        o[1] = pk_bf16(bf_lo(xv2[1]) + bf_lo(gv2[1]) * a0[2], bf_hi(xv2[1]) + bf_hi(gv2[1]) * a0[3]);
        o[2] = pk_bf16(bf_lo(xv2[2]) + bf_lo(gv2[2]) * a1[0], bf_hi(xv2[2]) + bf_hi(gv2[2]) * a1[1]);
        o[3] = pk_bf16(bf_lo(xv2[3]) + bf_lo(gv2[3]) * a1[2], bf_hi(xv2[3]) + bf_hi(gv2[3]) * a1[3]);
        bst(rs, (unsigned)(OFF_X1 + (((size_t)(mt2 * 4 + fmf) * 16 + (nt2 * 2 + fp)) * 64 + l) * 16), o);
        float vals[8];
#pragma unroll
        for (int i = 0; i < 4; ++i) { vals[2 * i] = bf_lo(o[i]); vals[2 * i + 1] = bf_hi(o[i]); }
        float s = ((vals[0] + vals[1]) + (vals[2] + vals[3])) + ((vals[4] + vals[5]) + (vals[6] + vals[7])), m, dm;
        red2_add(s, 0.f, m, dm);
        m *= (1.f / 32.f);
        float q = 0.f, Q;
#pragma unroll
        for (int i = 0; i < 8; ++i) { const float d = vals[i] - m; q += d * d; }
        red2_add(q, 0.f, Q, dm);
        if (g == 0) bst64(rs, (unsigned)(OFF_ST + (((size_t)(mt2 * 4 + fmf) * 16 + nt2 * 2 + fp) * 16 + (l & 15)) * 8), (u32x2){f2u(m), f2u(Q)});
        TSB(3);
        DRAIN();
        __syncthreads();
        if (threadIdx.x == 0) set_flag(cnt + (CNT_CX + mt2) * 64 + nt2);
    }
    TSW(1);
    if (wv == 7) {
#pragma unroll
        for (int i = 0; i < B3_K; ++i) K3_LOADB(i);
    }
#pragma unroll
    for (int i = B3_K; i < B3_A; ++i) K3_LOADB(i);
    TSW(2);
    const unsigned wdoff = (unsigned)(OFF_WD + (((size_t)nt2 * 4 * KS_F + kq) * 64 + l) * 16);
    u32x4 wdf[11][4];
    const int last4 = kq < 3 ? 40 : 36;
#define K4_LOADW(j) do { const int i_ = (j) >> 2, f_ = (j) & 3; wdf[i_][f_] = bld(rs, wdoff + (f_ * KS_F + (i_ < 10 ? 4 * i_ : last4)) * 1024); } while (0)
    if (act3) {
        u32x4* sA = smem;
        if (wv == 6) {
            const int ma = (mf0 * 16) >> 6, mb = (mf0 * 16 + nmf * 16 - 1) >> 6;
            wait_cnt(cnt + (CNT_CX + ma) * 64, 8u);
            wait_cnt(cnt + (CNT_CX + mb) * 64, 8u);
        }
        __syncthreads();
        TSW(3);
        u32x4 xa[4][3];
        u32x2 stp[4];
#define K3_LDA(c) do { _Pragma("unroll") for (int i = 0; i < 3; ++i) { const int mf = 3 * mh + i, mfa = mf0 + (mf < nmf ? mf : 0); \
                xa[c][i] = bld(rs, (unsigned)(OFF_X1 + (((size_t)mfa * 16 + 4 * (c) + kq) * 64 + l) * 16)); } SB(); } while (0)
#define K3_WRA(c) do { _Pragma("unroll") for (int i = 0; i < 3; ++i) sA[((3 * mh + i) * 16 + 4 * (c) + kq) * 64 + l] = xa[c][i]; SB(); } while (0)
#define K3_RDA(buf, ks) do { _Pragma("unroll") for (int mf = 0; mf < 6; ++mf) af[buf][mf] = sA[(mf * 16 + (ks)) * 64 + l]; } while (0)
        K3_LDA(0); K3_LDA(1); K3_LDA(2); K3_LDA(3);
#pragma unroll
        for (int i = B3_A; i < 16; ++i) K3_LOADB(i);
        K3_WRA(0); K3_WRA(1); K3_WRA(2); K3_WRA(3);
        __syncthreads(); SB();
        TSB(4);
        f32x4 ag[6], au[6];
#pragma unroll
        for (int mf = 0; mf < 6; ++mf) { ag[mf] = zero; au[mf] = zero; }
        u32x4 af[2][6];
        f32x4 cpart;
        K3_RDA(0, 0);
#pragma unroll
        for (int ks = 0; ks < 16; ++ks) {
            if (ks == 2) {
                const int i = wv < 6 ? wv : 0, mfa = mf0 + (i < nmf ? i : 0);
#pragma unroll
                for (int j = 0; j < 4; ++j) stp[j] = bld64(rs, (unsigned)(OFF_ST + (((size_t)mfa * 16 + 4 * g + j) * 16 + (l & 15)) * 8));
                SB();
            }
            if (ks == 9) {
                float m, dummy, q;
                const float m0 = u2f(stp[0][0]), m1 = u2f(stp[1][0]), m2 = u2f(stp[2][0]), m3 = u2f(stp[3][0]);
                red2_add((m0 + m1) + (m2 + m3), 0.f, m, dummy);
                m *= 0.0625f;
                const float e0 = m0 - m, e1 = m1 - m, e2 = m2 - m, e3 = m3 - m;
                red2_add(((u2f(stp[0][1]) + u2f(stp[1][1])) + (u2f(stp[2][1]) + u2f(stp[3][1]))) + 32.f * ((e0 * e0 + e1 * e1) + (e2 * e2 + e3 * e3)), 0.f, q, dummy);
                if (g == 0 && wv < 6) sSt[wv * 16 + l] = (f32x2){rsqrtf(q * (1.f / D_) + 1e-5f), -m};
                SB();
            }
            if (ks == 11) { __syncthreads(); SB(); }
            if (ks == 12) {
                const unsigned co = (unsigned)(OFF_C12 + (size_t)(actw ? pair : ng * 8) * 1024 + (l & 15) * 64 + g * 16);
                cpart = __builtin_bit_cast(f32x4, bld(rs, co)); SB();
            }
            if (ks + 1 < 16) K3_RDA((ks + 1) & 1, ks + 1);
            SB();
#pragma unroll
            for (int mf = 0; mf < 6; ++mf) { ag[mf] = MFMA32(bg[ks], af[ks & 1][mf], ag[mf]); au[mf] = MFMA32(bu[ks], af[ks & 1][mf], au[mf]); }
            SB();
            if (ks >= 3 && ks < 15) { K4_LOADW(2 * (ks - 3)); K4_LOADW(2 * (ks - 3) + 1); SB(); }
        }
        TSW(4);
        f32x4 c1g, c2g, c1u, c2u;
#pragma unroll
        for (int j = 0; j < 4; ++j) {
            float v = cpart[j];
            v += u2f(__builtin_amdgcn_update_dpp(0, f2u(v), 0x124, 0xf, 0xf, false));
            v += u2f(__builtin_amdgcn_update_dpp(0, f2u(v), 0x128, 0xf, 0xf, false));
            c1g[j] = u2f(__builtin_amdgcn_update_dpp(0, f2u(v), 0x00, 0xf, 0xf, false));
            c2g[j] = u2f(__builtin_amdgcn_update_dpp(0, f2u(v), 0x55, 0xf, 0xf, false));
            c1u[j] = u2f(__builtin_amdgcn_update_dpp(0, f2u(v), 0xaa, 0xf, 0xf, false));
            c2u[j] = u2f(__builtin_amdgcn_update_dpp(0, f2u(v), 0xff, 0xf, 0xf, false));
        }
        if (actw) {
#pragma unroll
            for (int mp = 0; mp < 3; ++mp) {
                if (2 * mp < nmf) {
                    u32x4 ov;
#pragma unroll
                    for (int q = 0; q < 2; ++q) {
                        const int mf = 2 * mp + q;
                        const f32x2 st = sSt[mf * 16 + (l & 15)];
                        float hv[4];
#pragma unroll
                        for (int j = 0; j < 4; ++j) {
                            const float gq = (ag[mf][j] + st[1] * c1g[j]) * st[0] + c2g[j];
                            const float uq = (au[mf][j] + st[1] * c1u[j]) * st[0] + c2u[j];
                            hv[j] = gelu_tanh(gq) * uq;
                        }
                        ov[2 * q] = pk_bf16(hv[0], hv[1]); ov[2 * q + 1] = pk_bf16(hv[2], hv[3]);
                    }
                    bst(rs, (unsigned)(OFF_HH + (((size_t)((mf0 >> 1) + mp) * NPAIR + pair) * 64 + l) * 16), ov);
                }
            }
        }
        TSB(5);
        DRAIN();
        __syncthreads();
        if (threadIdx.x == 0) set_flag(cnt + (CNT_CH + mt3) * 64 + ng);
    } else {
#pragma unroll
        for (int j = 0; j < 24; ++j) K4_LOADW(j);
    }
    TSW(5);
#pragma unroll
    for (int j = 24; j < 44; ++j) K4_LOADW(j);
    SB();
    {
        if (wv == 7) {
            const int ma = (mt2 * 64) / 96, mb = (mt2 * 64 + 63) / 96;
            wait_cnt(cnt + (CNT_CH + ma) * 64, 11u);
            wait_cnt(cnt + (CNT_CH + mb) * 64, 11u);
        }
        __syncthreads();
        TSW(6);
        f32x4 acc[4][2];
#pragma unroll
        for (int a = 0; a < 4; ++a) { acc[a][0] = zero; acc[a][1] = zero; }
        u32x4 hf[11][2];
#define K4_LOADH(i) do { const unsigned o_ = (unsigned)(OFF_HH + (((size_t)(mt2 * 2 + mh) * NPAIR + 2 * (kq + ((i) < 10 ? 4 * (i) : last4))) * 64 + l) * 16); \
            hf[i][0] = bld(rs, o_); hf[i][1] = bld(rs, o_ + 1024); SB(); } while (0)
#pragma unroll
        for (int i = 0; i < 3; ++i) K4_LOADH(i);
        const u32x4 xv = bld(rs, (unsigned)(OFF_X1 + (((size_t)(mt2 * 4 + fmf) * 16 + (nt2 * 2 + fp)) * 64 + l) * 16));
        const u32x4 gv = *(const u32x4*)((const u16*)(ws + OFF_MOD) + 2560 + n02);
        SB();
#pragma unroll
        for (int i = 0; i < 11; ++i) {
            if (i + 3 < 11) K4_LOADH(i + 3);
            if (i < 10 || kq < 3) {
                const u32x4 h0 = {hf[i][0][0], hf[i][0][1], hf[i][1][0], hf[i][1][1]}, h1 = {hf[i][0][2], hf[i][0][3], hf[i][1][2], hf[i][1][3]};
#pragma unroll
                for (int nf = 0; nf < 4; ++nf) { acc[nf][0] = MFMA32(wdf[i][nf], h0, acc[nf][0]); acc[nf][1] = MFMA32(wdf[i][nf], h1, acc[nf][1]); }
            }
            SB();
        }
        f32x4* sR = (f32x4*)smem;
#pragma unroll
        for (int nf = 0; nf < 4; ++nf)
#pragma unroll
            for (int mf = 0; mf < 2; ++mf) sR[(wv * 8 + nf * 2 + mf) * 64 + l] = acc[nf][mf];
        __syncthreads();
        f32x4 a0 = zero, a1 = zero;
#pragma unroll
        for (int ww = 0; ww < 4; ++ww) {
            a0 += sR[((mh * 4 + ww) * 8 + (2 * fp) * 2 + (kq >> 1)) * 64 + l];
            a1 += sR[((mh * 4 + ww) * 8 + (2 * fp + 1) * 2 + (kq >> 1)) * 64 + l];
        }
        u32x4 o;
        o[0] = pk_bf16(bf_lo(xv[0]) + bf_lo(gv[0]) * a0[0], bf_hi(xv[0]) + bf_hi(gv[0]) * a0[1]);
        o[1] = pk_bf16(bf_lo(xv[1]) + bf_lo(gv[1]) * a0[2], bf_hi(xv[1]) + bf_hi(gv[1]) * a0[3]);
        o[2] = pk_bf16(bf_lo(xv[2]) + bf_lo(gv[2]) * a1[0], bf_hi(xv[2]) + bf_hi(gv[2]) * a1[1]);
        o[3] = pk_bf16(bf_lo(xv[3]) + bf_lo(gv[3]) * a1[2], bf_hi(xv[3]) + bf_hi(gv[3]) * a1[3]);
        *(u32x4*)(out + (size_t)tok2 * D_ + n02) = o;
    }
    TSW_FLUSH(2);
    TS_FLUSH(1);
}

extern "C" void kernel_launch(void* const* d_in, const int* in_sizes, int n_in,
                              void* d_out, int out_size, void* d_ws, size_t ws_size,
                              hipStream_t stream) {
    const u16* x = (const u16*)d_in[0];
    const u16* wqkv = (const u16*)d_in[1];
    const u16* wphi = (const u16*)d_in[2];
    const u16* wo = (const u16*)d_in[3];
    const u16* wg = (const u16*)d_in[5];
    const u16* wu = (const u16*)d_in[6];
    const u16* wd = (const u16*)d_in[7];
    const u16* cvec = (const u16*)d_in[8];
    const u16* wmod = (const u16*)d_in[9];
    char* ws = (char*)d_ws;
    k0_prep<<<K0_GRID, 256, 0, stream>>>(x, wqkv, wphi, cvec, wmod, wo, ws);
    kf_main<<<256, 512, 0, stream>>>(x, wg, wu, wd, ws, (u16*)d_out);
}
```

```cpp
#include <hip/hip_runtime.h>
#include <hip/hip_bf16.h>
#include <stdint.h>

typedef short bf16x8 __attribute__((ext_vector_type(8)));
typedef short bf16x4 __attribute__((ext_vector_type(4)));
typedef float f32x4 __attribute__((ext_vector_type(4)));
typedef float f32x2 __attribute__((ext_vector_type(2)));
typedef unsigned u32x4 __attribute__((ext_vector_type(4)));
typedef unsigned u32x2 __attribute__((ext_vector_type(2)));
typedef __bf16 bf16x2_t __attribute__((ext_vector_type(2)));
typedef unsigned short u16;

#define PT_ST 2
#define K3_LA 4
#define DI __device__ __forceinline__
#define MFMA32(a, b, c) __builtin_amdgcn_mfma_f32_16x16x32_bf16(__builtin_bit_cast(bf16x8, (a)), __builtin_bit_cast(bf16x8, (b)), (c), 0, 0, 0)
#define SB() __builtin_amdgcn_sched_barrier(0)

constexpr int S_ = 2048, D_ = 512, H_ = 32, F_ = 1376;
constexpr int NMF = S_ / 16;
constexpr int KS_D = D_ / 32;
constexpr int KS_F = F_ / 32;
constexpr int NPAIR = F_ / 16;
constexpr float LOG2E = 1.4426950408889634f;

constexpr size_t OFF_MOD  = 0;
constexpr size_t OFF_CNT  = 8192;
constexpr size_t OFF_KVF  = OFF_CNT + 49152;
constexpr size_t OFF_ZF   = OFF_KVF + 65536;
constexpr size_t OFF_SLAB = OFF_ZF + 4096;
constexpr size_t OFF_ZSL  = OFF_SLAB + (size_t)32 * 32 * 1024;
constexpr size_t OFF_XN   = OFF_ZSL + (size_t)32 * 32 * 128;
constexpr size_t OFF_WQKV = OFF_XN + (size_t)S_ * D_ * 2;
constexpr size_t OFF_WO   = OFF_WQKV + (size_t)96 * 16384;
constexpr size_t OFF_WGU  = OFF_WO + (size_t)32 * 16384;
constexpr size_t OFF_WD   = OFF_WGU + (size_t)88 * 2 * 16384;
constexpr size_t OFF_PHIQ = OFF_WD + (size_t)32 * KS_F * 1024;
constexpr size_t OFF_X1   = OFF_PHIQ + (size_t)NMF * 32 * 1024;
constexpr size_t OFF_ST   = OFF_X1 + (size_t)S_ * D_ * 2;
constexpr size_t OFF_C12  = OFF_ST + (size_t)S_ * 16 * 8;
constexpr size_t OFF_WGU2 = OFF_C12 + (size_t)88 * 1024;
constexpr size_t OFF_HH   = OFF_WGU2 + (size_t)88 * 2 * 16384;

DI unsigned pk_bf16(float a, float b) {
    f32x2 v = {a, b};
    bf16x2_t r = __builtin_convertvector(v, bf16x2_t);
    return __builtin_bit_cast(unsigned, r);
}
DI float bf_lo(unsigned u) { return __uint_as_float(u << 16); }
DI float bf_hi(unsigned u) { return __uint_as_float(u & 0xffff0000u); }
DI float bf_round(float a) { return bf_lo(pk_bf16(a, 0.f)); }
DI float fexp2(float x) { return __builtin_amdgcn_exp2f(x); }
DI float frcp(float x) { return __builtin_amdgcn_rcpf(x); }

template <int KMAP, int NMAP>
DI void pretile_wave(const u16* __restrict__ W, int ldw, int ncols, int ks, int cgrp, u32x4* __restrict__ dst, int nks, int nf_mul, int nf_add,
                     u32x4* sm  ) {
    const int l = threadIdx.x & 63, g = l >> 4, c8 = l & 15;
    const int col0 = cgrp * 128 + c8 * 8;
    const bool ok = col0 < ncols;
    const int colc = ok ? col0 : 0;
    u32x4 r[8];
#pragma unroll
    for (int j = 0; j < 8; ++j) {
        const int k = ks * 32 + (KMAP == 0 ? 8 * g + j : 16 * (j >> 2) + 4 * g + (j & 3));
        r[j] = *(const u32x4*)(W + (size_t)k * ldw + colc);
    }
#pragma unroll
    for (int c = 0; c < 8; ++c) {
        u32x4 o;
#pragma unroll
        for (int jj = 0; jj < 4; ++jj) {
            const unsigned a = r[2 * jj][c >> 1], b = r[2 * jj + 1][c >> 1];
            o[jj] = (c & 1) ? ((a >> 16) | (b & 0xffff0000u)) : ((a & 0xffffu) | (b << 16));
        }
        const int n = c8 * 8 + c;
        int nf, rr;
        if (NMAP == 0) { nf = n >> 4; rr = n & 15; }
        else { const int cc = n & 31; nf = 2 * (n >> 5) + ((cc >> 2) & 1); rr = 4 * (cc >> 3) + (cc & 3); }
        sm[nf * 65 + g * 16 + rr] = o;
    }
    asm volatile("s_waitcnt lgkmcnt(0)" ::: "memory");
    __builtin_amdgcn_wave_barrier();
#pragma unroll
    for (int nf = 0; nf < 8; ++nf) {
        const int nfg = cgrp * 8 + nf;
        if (nfg * 16 < ncols) {
            const u32x4 o = sm[nf * 65 + l];
            u32x4* p_ = &dst[((size_t)(nfg * nf_mul + nf_add) * nks + ks) * 64 + l];
            asm volatile("global_store_dwordx4 %0, %1, off sc1\n\ts_nop 1" :: "v"(p_), "v"(o) : "memory");
        }
    }
}

DI float u2f(unsigned u) { return __uint_as_float(u); }
DI unsigned f2u(float f) { return __float_as_uint(f); }
DI void red2_max(float a, float b, float& A, float& B) {
    auto r = __builtin_amdgcn_permlane32_swap(f2u(a), f2u(b), false, false);
    const float c = fmaxf(u2f(r[0]), u2f(r[1]));
    auto r2 = __builtin_amdgcn_permlane16_swap(f2u(c), f2u(c), false, false);
    const float d = fmaxf(u2f(r2[0]), u2f(r2[1]));
    auto r3 = __builtin_amdgcn_permlane32_swap(f2u(d), f2u(d), false, false);
    A = u2f(r3[0]); B = u2f(r3[1]);
}
DI void red2_add(float a, float b, float& A, float& B) {
    auto r = __builtin_amdgcn_permlane32_swap(f2u(a), f2u(b), false, false);
    const float c = u2f(r[0]) + u2f(r[1]);
    auto r2 = __builtin_amdgcn_permlane16_swap(f2u(c), f2u(c), false, false);
    const float d = u2f(r2[0]) + u2f(r2[1]);
    auto r3 = __builtin_amdgcn_permlane32_swap(f2u(d), f2u(d), false, false);
    A = u2f(r3[0]); B = u2f(r3[1]);
}

constexpr int K0_MOD = 96, K0_XN = 128, K0_FOLD = 64, K0_WV = 16, K0_WO = 16, K0_WG = 0, K0_WU = 0, K0_WD = 0;
constexpr int K0_B0 = K0_MOD, K0_B1 = K0_B0 + K0_XN, K0_B2 = K0_B1 + K0_FOLD, K0_B3 = K0_B2 + K0_WV;
constexpr int K0_B4 = K0_B3 + K0_WO, K0_B5 = K0_B4 + K0_WG, K0_B6 = K0_B5 + K0_WU, K0_B7 = K0_B6 + K0_WD;
constexpr int K0_GRID = K0_B7;

__global__ __launch_bounds__(256) void k0_prep(const u16* __restrict__ x, const u16* __restrict__ wqkv, const u16* __restrict__ wphi,
                                               const u16* __restrict__ cvec, const u16* __restrict__ wmod,
                                               const u16* __restrict__ wo, char* __restrict__ ws) {
    __shared__ __attribute__((aligned(16))) u32x4 smT[4 * 520];
    float* red = (float*)smT;
    const int pb = blockIdx.x, t = threadIdx.x;
    const int b = pb < 160 ? pb + 96 : (pb < 256 ? pb - 160 : pb);
    if (b >= K0_B0 && b < K0_B0 + 12) ((u32x4*)(ws + OFF_CNT))[(b - K0_B0) * 256 + t] = (u32x4){0u, 0u, 0u, 0u};
    if (b < K0_B0) {
        const int cb = (b & 7) * 12 + (b >> 3);
        const int cgp = t & 3, kg = t >> 2;
        float acc[8];
#pragma unroll
        for (int i = 0; i < 8; ++i) acc[i] = 0.f;
        u32x4 wv[8]; float sc[8];
        const unsigned c2 = ((const unsigned*)cvec)[t];
#pragma unroll
        for (int i = 0; i < 8; ++i) wv[i] = *(const u32x4*)(wmod + (size_t)(kg + 64 * i) * 3072 + cb * 32 + cgp * 8);
        SB();
        {
            const float c0 = bf_lo(c2), c1 = bf_hi(c2);
            *(f32x2*)(red + 256 + 2 * t) = (f32x2){bf_round(c0 / (1.f + expf(-c0))), bf_round(c1 / (1.f + expf(-c1)))};
        }
        __syncthreads();
#pragma unroll
        for (int i = 0; i < 8; ++i) sc[i] = red[256 + kg + 64 * i];
#pragma unroll
        for (int i = 0; i < 8; ++i) {
#pragma unroll
            for (int j = 0; j < 4; ++j) {
                acc[2 * j] += sc[i] * bf_lo(wv[i][j]);
                acc[2 * j + 1] += sc[i] * bf_hi(wv[i][j]);
            }
        }
#pragma unroll
        for (int j = 0; j < 8; ++j) {
            acc[j] += u2f(__builtin_amdgcn_update_dpp(0, f2u(acc[j]), 0x124, 0xf, 0xf, false));
            acc[j] += u2f(__builtin_amdgcn_update_dpp(0, f2u(acc[j]), 0x128, 0xf, 0xf, false));
        }
#pragma unroll
        for (int j = 0; j < 8; j += 2) red2_add(acc[j], acc[j + 1], acc[j], acc[j + 1]);
        if ((t & 63) < 4) {
#pragma unroll
            for (int j = 0; j < 8; ++j) red[(t >> 6) * 32 + cgp * 8 + j] = acc[j];
        }
        __syncthreads();
        if (t < 32) {
            float s = bf_round((red[t] + red[32 + t]) + (red[64 + t] + red[96 + t]));
            const int n = cb * 32 + t, row = n >> 9;
            if (row == 1 || row == 4) s = 1.f + s;
            ((u16*)(ws + OFF_MOD))[n] = (u16)(pk_bf16(s, 0.f) & 0xffffu);
        }
    } else if (b < K0_B1) {
        const int mf = b - K0_B0, r = t & 15, c = t >> 4;
        const u16* xr = x + (size_t)(mf * 16 + r) * D_;
        u32x4 v[4];
#pragma unroll
        for (int i = 0; i < 4; ++i) v[i] = *(const u32x4*)(xr + (c + 16 * i) * 8);
        float f[32];
        float s = 0.f;
#pragma unroll
        for (int i = 0; i < 4; ++i)
#pragma unroll
            for (int j = 0; j < 4; ++j) { f[i * 8 + 2 * j] = bf_lo(v[i][j]); f[i * 8 + 2 * j + 1] = bf_hi(v[i][j]); s += f[i * 8 + 2 * j] + f[i * 8 + 2 * j + 1]; }
        s += __shfl_xor(s, 16); s += __shfl_xor(s, 32);
        if ((t & 63) < 16) red[(t >> 6) * 16 + r] = s;
        __syncthreads();
        const float m = ((red[r] + red[16 + r]) + (red[32 + r] + red[48 + r])) * (1.f / D_);
        float q = 0.f;
#pragma unroll
        for (int i = 0; i < 32; ++i) { f[i] -= m; q += f[i] * f[i]; }
        q += __shfl_xor(q, 16); q += __shfl_xor(q, 32);
        if ((t & 63) < 16) red[64 + (t >> 6) * 16 + r] = q;
        __syncthreads();
        const float var = (red[64 + r] + red[80 + r]) + (red[96 + r] + red[112 + r]);
        const float rs = rsqrtf(var * (1.f / D_) + 1e-5f);
        u32x4* dst = (u32x4*)(ws + OFF_XN) + (size_t)mf * 16 * 64;
#pragma unroll
        for (int i = 0; i < 4; ++i) {
            u32x4 o;
#pragma unroll
            for (int j = 0; j < 4; ++j) o[j] = pk_bf16(f[i * 8 + 2 * j] * rs, f[i * 8 + 2 * j + 1] * rs);
            const int ch = c + 16 * i;
            dst[(ch >> 2) * 64 + (ch & 3) * 16 + r] = o;
        }
    } else if (b < K0_B2) {
        const int wv = (b - K0_B1) * 4 + (t >> 6);
        const int hq = wv >> 2, kq = wv & 3;
        const int l = t & 63, r = l & 15, g = l >> 4, h = hq & 31;
        bf16x4 bphi;
#pragma unroll
        for (int j = 0; j < 4; ++j) bphi[j] = (short)wphi[h * 256 + (4 * g + j) * 16 + r];
        char* dst = ws + OFF_WQKV + (size_t)hq * 16384;
        bf16x4 a[8];
#pragma unroll
        for (int i = 0; i < 8; ++i) a[i] = *(const bf16x4*)(wqkv + (size_t)((kq * 8 + i) * 16 + r) * 1536 + hq * 16 + 4 * g);
#pragma unroll
        for (int i = 0; i < 8; ++i) {
            const int rb = kq * 8 + i;
            f32x4 d = {0.f, 0.f, 0.f, 0.f};
            d = __builtin_amdgcn_mfma_f32_16x16x16bf16_1k(a[i], bphi, d, 0, 0, 0);
            u32x2 o;
            o[0] = pk_bf16(d[0] * LOG2E, d[1] * LOG2E);
            o[1] = pk_bf16(d[2] * LOG2E, d[3] * LOG2E);
            const int ks = rb >> 1, gp = 2 * (rb & 1) + (g >> 1);
            *(u32x2*)(dst + ks * 1024 + (gp * 16 + r) * 16 + (g & 1) * 8) = o;
        }
    } else {
        u32x4* sm = smT + (t >> 6) * 520;
        if (b < K0_B3) {
            const int id = (b - K0_B2) * 4 + (t >> 6);
            pretile_wave<0, 0>(wqkv + 1024, 1536, 512, id >> 2, id & 3, (u32x4*)(ws + OFF_WQKV), KS_D, 1, 64, sm);
        } else {
            const int id = (b - K0_B3) * 4 + (t >> 6);
            pretile_wave<1, 1>(wo, 512, 512, id >> 2, id & 3, (u32x4*)(ws + OFF_WO), KS_D, 1, 0, sm);
        }
    }
}

DI float fmax2(float a, float b) { return __builtin_amdgcn_fmed3f(a, b, __builtin_inff()); }
DI float fmin2(float a, float b) { return __builtin_amdgcn_fmed3f(a, b, -__builtin_inff()); }
template <int N>
DI void softmax_pm(const f32x4 (&p)[N], u32x4 (&o)[N]) {
    float a[N], b[N];
#pragma unroll
    for (int i = 0; i < N; ++i) {
        a[i] = fmax2(fmax2(p[i][0], p[i][1]), fmax2(p[i][2], p[i][3]));
        b[i] = -fmin2(fmin2(p[i][0], p[i][1]), fmin2(p[i][2], p[i][3]));
    }
#pragma unroll
    for (int i = 0; i < N; ++i) { auto r = __builtin_amdgcn_permlane32_swap(f2u(a[i]), f2u(b[i]), false, false); a[i] = fmax2(u2f(r[0]), u2f(r[1])); }
#pragma unroll
    for (int i = 0; i < N; ++i) { auto r = __builtin_amdgcn_permlane16_swap(f2u(a[i]), f2u(a[i]), false, false); a[i] = fmax2(u2f(r[0]), u2f(r[1])); }
#pragma unroll
    for (int i = 0; i < N; ++i) { auto r = __builtin_amdgcn_permlane32_swap(f2u(a[i]), f2u(a[i]), false, false); a[i] = u2f(r[0]); b[i] = u2f(r[1]); }
    float ep[N][4], en[N][4];
#pragma unroll
    for (int i = 0; i < N; ++i)
#pragma unroll
        for (int j = 0; j < 4; ++j) { ep[i][j] = fexp2(p[i][j] - a[i]); en[i][j] = fexp2(-b[i] - p[i][j]); }
#pragma unroll
    for (int i = 0; i < N; ++i) { a[i] = (ep[i][0] + ep[i][1]) + (ep[i][2] + ep[i][3]); b[i] = (en[i][0] + en[i][1]) + (en[i][2] + en[i][3]); }
#pragma unroll
    for (int i = 0; i < N; ++i) { auto r = __builtin_amdgcn_permlane32_swap(f2u(a[i]), f2u(b[i]), false, false); a[i] = u2f(r[0]) + u2f(r[1]); }
#pragma unroll
    for (int i = 0; i < N; ++i) { auto r = __builtin_amdgcn_permlane16_swap(f2u(a[i]), f2u(a[i]), false, false); a[i] = frcp(u2f(r[0]) + u2f(r[1])); }
#pragma unroll
    for (int i = 0; i < N; ++i) { auto r = __builtin_amdgcn_permlane32_swap(f2u(a[i]), f2u(a[i]), false, false); a[i] = u2f(r[0]); b[i] = u2f(r[1]); }
#pragma unroll
    for (int i = 0; i < N; ++i) {
        o[i][0] = pk_bf16(ep[i][0] * a[i], ep[i][1] * a[i]); o[i][1] = pk_bf16(ep[i][2] * a[i], ep[i][3] * a[i]);
        o[i][2] = pk_bf16(en[i][0] * b[i], en[i][1] * b[i]); o[i][3] = pk_bf16(en[i][2] * b[i], en[i][3] * b[i]);
    }
}

DI u32x4 modulate8(const u32x4 xv, const float (&gg)[8], const float (&ss)[8]) {
    u32x4 o;
#pragma unroll
    for (int j = 0; j < 4; ++j)
        o[j] = pk_bf16(bf_lo(xv[j]) * gg[2 * j] + ss[2 * j], bf_hi(xv[j]) * gg[2 * j + 1] + ss[2 * j + 1]);
    return o;
}

#define RLX_AGENT __ATOMIC_RELAXED, __HIP_MEMORY_SCOPE_AGENT
constexpr int CNT_CK = 0, CNT_CV = 32, CNT_CP = 64, CNT_CX = 96, CNT_CH = 128, CNT_CW = 150, CNT_CT = 170, CNT_CD = 172;
typedef __amdgpu_buffer_rsrc_t rsrc_t;
DI u32x4 bld(rsrc_t rs, unsigned off) { return __builtin_amdgcn_raw_buffer_load_b128(rs, off, 0, 16); }
DI u32x2 bld64(rsrc_t rs, unsigned off) { return __builtin_amdgcn_raw_buffer_load_b64(rs, off, 0, 16); }
DI void bst(rsrc_t rs, unsigned off, u32x4 v) { __builtin_amdgcn_raw_buffer_store_b128(v, rs, off, 0, 16); }
DI void bst64(rsrc_t rs, unsigned off, u32x2 v) { __builtin_amdgcn_raw_buffer_store_b64(v, rs, off, 0, 16); }
#define POLL_SLEEP 4
#define DRAIN() asm volatile("s_waitcnt vmcnt(0)" ::: "memory")
DI void wait_cnt(unsigned* c, unsigned n) {
    unsigned sp = 0;
    const unsigned lane = threadIdx.x & 63;
    unsigned* p = c + (lane < n ? lane : 0);
    while (!__all(__hip_atomic_load(p, RLX_AGENT) != 0u)) { __builtin_amdgcn_s_sleep(POLL_SLEEP); if (++sp > (1u << 18)) break; }
    __builtin_amdgcn_fence(__ATOMIC_ACQUIRE, "wavefront");
    asm volatile("" ::: "memory");
}
DI void set_flag(unsigned* c) { __hip_atomic_store(c, 1u, RLX_AGENT); }
DI float gelu_tanh(float g) {
    const float k0 = -2.f * 0.7978845608028654f * LOG2E, k1 = k0 * 0.044715f;
    const float a = g * (g * g * k1 + k0);
    return g * frcp(1.f + fexp2(a));
}
DI u32x4 scale8(const u32x4 v, const u32x4 gv) {
    u32x4 o;
#pragma unroll
    for (int j = 0; j < 4; ++j) o[j] = pk_bf16(bf_lo(v[j]) * bf_lo(gv[j]), bf_hi(v[j]) * bf_hi(gv[j]));
    return o;
}

#define B3_EARLY 10
#define B3_K 12
#define B3_A 16
#define B3_PER 0
#define B3_MID 12
extern "C" __global__ __launch_bounds__(512, 2) void kf_main(const u16* __restrict__ x, const u16* __restrict__ wg, const u16* __restrict__ wu, const u16* __restrict__ wd,
                                                                char* __restrict__ ws, u16* __restrict__ out) {
    __shared__ __attribute__((aligned(16))) u32x4 smem[9216 + 128 + 128 + 64 + 128 + 4];
    const int xcd = blockIdx.x & 7, bi = blockIdx.x >> 3;
    const int wv = __builtin_amdgcn_readfirstlane(threadIdx.x >> 6);
    const int l = threadIdx.x & 63, g = l >> 4;
    unsigned* cnt = (unsigned*)(ws + OFF_CNT);
    const rsrc_t rs = __builtin_amdgcn_make_buffer_rsrc(ws, 0, 0x4000000, 0x00020000);
    u32x4* sWO = smem + 5120;
    u32x4* sZ = smem + 9216;
    u32x4* sGB = smem + 9216 + 128;
    f32x2* sSt = (f32x2*)(smem + 9216 + 256);
    volatile unsigned* sFlag = (volatile unsigned*)(smem + 9216 + 448);
    const u32x4 ones = {0x3f803f80u, 0x3f803f80u, 0x3f803f80u, 0x3f803f80u};
    const f32x4 zero = {0.f, 0.f, 0.f, 0.f};

    const int mt2 = 4 * xcd + (bi >> 3), nt2 = bi & 7;
    const int kq = wv & 3, mh = wv >> 2;
    const int t0_ = (242 * xcd) >> 3, t1_ = (242 * (xcd + 1)) >> 3;
    const int id3 = t0_ + bi;
    const bool act3 = id3 < t1_;
    const int id3c = act3 ? id3 : t0_;
    const int ng = (id3c % 121) / 11, mt3 = 11 * (id3c / 121) + (id3c % 121) % 11;
    const int mf0 = mt3 * 6;
    const int nmf = (NMF - mf0) < 6 ? (NMF - mf0) : 6;
    const int pair = ng * 8 + wv;
    const bool actw = pair < NPAIR;
    const unsigned wgoff = (unsigned)(OFF_WGU2 + ((size_t)(actw ? pair : ng * 8) * 2048 + l) * 16);
    u32x4 bg[16], bu[16];
#define K3_LOADB(ks) do { bg[ks] = bld(rs, wgoff + (ks) * 1024); bu[ks] = bld(rs, wgoff + 16384 + (ks) * 1024); SB(); } while (0)

#define WO_DMA(part) do { const u32x4* WO_ = (const u32x4*)(ws + OFF_WO) + ((size_t)nt2 * 64 + (part) * 8) * 64 + l; \
        const unsigned ldsb_ = (unsigned)(size_t)(sWO + (part) * 8 * 64); \
        _Pragma("unroll") for (int i_ = 0; i_ < 8; ++i_) { unsigned keep_; \
            asm volatile("s_mov_b32 %0, m0\n\ts_mov_b32 m0, %2\n\ts_nop 0\n\tglobal_load_lds_dwordx4 %1, off\n\ts_mov_b32 m0, %0" : "=&s"(keep_) : "v"(WO_ + i_ * 64), "s"(ldsb_ + i_ * 1024) : "memory"); } } while (0)
    if (threadIdx.x < 16) sFlag[threadIdx.x] = 0u;
    {
        u32x4* sA = smem;
        char* sP = (char*)(smem + 4096);
        const int mt = 8 * (xcd >> 1) + (bi >> 2), hg = 4 * (xcd & 1) + (bi & 3);
        const int w = wv & 3;
        const int h = hg * 4 + w;
        const u32x4* WQ = (const u32x4*)(ws + OFF_WQKV) + (size_t)h * 1024 + l;
        u32x4 af[2][4];
#define K1_RDA(buf, ks) do { _Pragma("unroll") for (int mf = 0; mf < 4; ++mf) af[buf][mf] = sA[(mf * 16 + (ks)) * 64 + l]; } while (0)
        const u32x4* XN = (const u32x4*)(ws + OFF_XN) + (size_t)mt * 4096 + mh * 2048 + w * 64 + l;
        const u32x4* MS = (const u32x4*)(ws + OFF_MOD) + w * 4 + g;
        const u32x4* WB = WQ + (wv < 4 ? 32 : 64) * 1024;
        u32x4 xa[4][2], gs[4][2], bb[16];
#define K1_LOADA(c) do { gs[c][0] = MS[64 + (c) * 16]; gs[c][1] = MS[(c) * 16]; \
        _Pragma("unroll") for (int mf = 0; mf < 2; ++mf) xa[c][mf] = XN[mf * 1024 + (c) * 256]; SB(); } while (0)
#define K1_PREP1(c, mf) do { float gg[8], ss[8]; \
        _Pragma("unroll") for (int j = 0; j < 4; ++j) { gg[2 * j] = bf_lo(gs[c][0][j]); gg[2 * j + 1] = bf_hi(gs[c][0][j]); ss[2 * j] = bf_lo(gs[c][1][j]); ss[2 * j + 1] = bf_hi(gs[c][1][j]); } \
        sA[((2 * mh + (mf)) * 16 + 4 * (c) + w) * 64 + l] = modulate8(xa[c][mf], gg, ss); } while (0)
#define K1_LOADB(ks) do { bb[ks] = WB[(ks) * 64]; } while (0)
        f32x4 ac[4];
#pragma unroll
        for (int mf = 0; mf < 4; ++mf) ac[mf] = zero;
        K1_LOADA(0); K1_LOADA(1);
        K1_LOADB(0); K1_LOADB(1); K1_LOADB(2); K1_LOADB(3); SB();
        K1_LOADA(2); K1_LOADA(3);
        K1_LOADB(4); K1_LOADB(5); K1_LOADB(6); K1_LOADB(7); SB();
        K1_PREP1(0, 0); K1_PREP1(0, 1); K1_PREP1(1, 0); K1_PREP1(1, 1);
        __syncthreads(); SB();
        u32x4 bq[16];
        u32x4 a3[3][4];
#define K1_RD3(ks) do { _Pragma("unroll") for (int mf = 0; mf < 4; ++mf) a3[(ks) % 3][mf] = sA[(mf * 16 + (ks)) * 64 + l]; } while (0)
        K1_RD3(0); K1_RD3(1);
#pragma unroll
        for (int ks = 0; ks < 16; ++ks) {
            if (ks + 8 < 16) K1_LOADB(ks + 8);
            else if (wv >= 4) bq[ks - 8] = WQ[(ks - 8) * 64];
            if (ks == 6) { __syncthreads(); }
            if (ks + 2 < 16) K1_RD3(ks + 2);
#pragma unroll
            for (int mf = 0; mf < 4; ++mf) ac[mf] = MFMA32(bb[ks], a3[ks % 3][mf], ac[mf]);
            if (ks == 0) K1_PREP1(2, 0);
            if (ks == 1) K1_PREP1(2, 1);
            if (ks == 2) K1_PREP1(3, 0);
            if (ks == 3) K1_PREP1(3, 1);
            SB();
        }
        char* sV = (char*)(sWO + 3584);
        if (wv < 4) {
            __builtin_amdgcn_s_setprio(3);
            char* myP = sP + w * 4096;
            {
                u32x4 o[4];
                softmax_pm<4>(ac, o);
#pragma unroll
                for (int mf = 0; mf < 4; ++mf) {
                    char* row = myP + (mf * 16 + (l & 15)) * 64;
                    *(u32x2*)(row + g * 8) = (u32x2){o[mf][0], o[mf][1]};
                    *(u32x2*)(row + 32 + g * 8) = (u32x2){o[mf][2], o[mf][3]};
                }
            }
            const unsigned base = (unsigned)(size_t)myP + (4 * g + ((l & 15) >> 2)) * 64 + (l & 3) * 8;
            u32x2 t0, t1, t2, t3, t4, t5, t6, t7;
            asm volatile("s_waitcnt lgkmcnt(0)\n\t"
                         "ds_read_b64_tr_b16 %0, %8\n\tds_read_b64_tr_b16 %1, %8 offset:1024\n\t"
                         "ds_read_b64_tr_b16 %2, %8 offset:32\n\tds_read_b64_tr_b16 %3, %8 offset:1056\n\t"
                         "ds_read_b64_tr_b16 %4, %8 offset:2048\n\tds_read_b64_tr_b16 %5, %8 offset:3072\n\t"
                         "ds_read_b64_tr_b16 %6, %8 offset:2080\n\tds_read_b64_tr_b16 %7, %8 offset:3104\n\t"
                         "s_waitcnt lgkmcnt(0)"
                         : "=&v"(t0), "=&v"(t1), "=&v"(t2), "=&v"(t3), "=&v"(t4), "=&v"(t5), "=&v"(t6), "=&v"(t7) : "v"(base) : "memory");
            if (l == 0) __hip_atomic_fetch_add((unsigned*)&sFlag[2], 16u, __ATOMIC_RELAXED, __HIP_MEMORY_SCOPE_WORKGROUP);
            const u32x4 pa00 = {t0[0], t0[1], t1[0], t1[1]}, pa01 = {t2[0], t2[1], t3[0], t3[1]};
            const u32x4 pa10 = {t4[0], t4[1], t5[0], t5[1]}, pa11 = {t6[0], t6[1], t7[0], t7[1]};
            { unsigned sp = 0; while (sFlag[8 + w] == 0u) { __builtin_amdgcn_s_sleep(1); if (++sp > (1u << 22)) break; } }
            asm volatile("" ::: "memory");
            u32x4 vb0, vb1;
            {
                const unsigned vbase = (unsigned)(size_t)sV + w * 2048 + (4 * g + ((l & 15) >> 2)) * 32 + (l & 3) * 8;
                u32x2 v0, v1, v2, v3;
                asm volatile("ds_read_b64_tr_b16 %0, %4\n\tds_read_b64_tr_b16 %1, %4 offset:512\n\t"
                             "ds_read_b64_tr_b16 %2, %4 offset:1024\n\tds_read_b64_tr_b16 %3, %4 offset:1536\n\ts_waitcnt lgkmcnt(0)"
                             : "=&v"(v0), "=&v"(v1), "=&v"(v2), "=&v"(v3) : "v"(vbase) : "memory");
                vb0 = (u32x4){v0[0], v0[1], v1[0], v1[1]}; vb1 = (u32x4){v2[0], v2[1], v3[0], v3[1]};
            }
            f32x4 kv0 = MFMA32(pa00, vb0, zero), kv1 = MFMA32(pa01, vb0, zero);
            f32x4 z0 = MFMA32(pa00, ones, zero), z1 = MFMA32(pa01, ones, zero);
            kv0 = MFMA32(pa10, vb1, kv0); kv1 = MFMA32(pa11, vb1, kv1);
            z0 = MFMA32(pa10, ones, z0); z1 = MFMA32(pa11, ones, z1);
            u32x4 o;
            o[0] = pk_bf16(kv0[0], kv0[1]); o[1] = pk_bf16(kv0[2], kv0[3]); o[2] = pk_bf16(kv1[0], kv1[1]); o[3] = pk_bf16(kv1[2], kv1[3]);
            bst(rs, (unsigned)(OFF_SLAB + (((size_t)h * 32 + mt) * 64 + l) * 16), o);
            if ((l & 15) < 2) {
                const f32x4 zz = (l & 1) ? z1 : z0;
                bst(rs, (unsigned)(OFF_ZSL + (((size_t)h * 32 + mt) * 8 + g * 2 + (l & 1)) * 16), __builtin_bit_cast(u32x4, zz));
            }
            DRAIN();
            if (l == 0) set_flag(cnt + (CNT_CK + h) * 64 + mt);
            __builtin_amdgcn_s_setprio(0);
            if (l == 0) __hip_atomic_fetch_add((unsigned*)&sFlag[5], 1u, __ATOMIC_RELAXED, __HIP_MEMORY_SCOPE_WORKGROUP);
            const bool dunit = wv == 0 && blockIdx.x < 172;
            const bool wunit = (wv == 1 || wv == 2) && blockIdx.x >= 84;
            const int wmat = wv - 1, wcg = ((int)blockIdx.x - 84) >> 2, wkq = blockIdx.x & 3;
            u32x4 rr[1][8], wgam[4], wbet[4];
            if (dunit) {
                const int ks = blockIdx.x >> 2, cgrp = blockIdx.x & 3;
#pragma unroll
                for (int j = 0; j < 8; ++j) rr[0][j] = *(const u32x4*)(wd + (size_t)(ks * 32 + 16 * (j >> 2) + 4 * g + (j & 3)) * 512 + cgrp * 128 + (l & 15) * 8);
            }
            if (wunit) {
                const u16* W = wmat ? wu : wg;
#pragma unroll
                for (int j = 0; j < 8; ++j)
                    rr[0][j] = *(const u32x4*)(W + (size_t)((wkq * 4 + (l >> 4)) * 32 + 8 * ((l >> 2) & 3) + j) * F_ + wcg * 32 + (l & 3) * 8);
#pragma unroll
                for (int i = 0; i < 4; ++i) {
                    wgam[i] = ((const u32x4*)(ws + OFF_MOD))[256 + (wkq * 4 + i) * 4 + g];
                    wbet[i] = ((const u32x4*)(ws + OFF_MOD))[192 + (wkq * 4 + i) * 4 + g];
                }
            }
            SB();
            if (wv == 3) {
                { unsigned sp = 0; while (sFlag[5] != 4u) { __builtin_amdgcn_s_sleep(1); if (++sp > (1u << 22)) break; } }
                WO_DMA(7); WO_DMA(6); WO_DMA(5); WO_DMA(4);
            }
            if (dunit || wunit) {
                { unsigned sp = 0; while (sFlag[6] != 4u || (sFlag[2] >> 4) != 4u) { __builtin_amdgcn_s_sleep(1); if (++sp > (1u << 22)) break; } }
                asm volatile("" ::: "memory");
            }

            u32x4* sm = smem + w * 1040;
            if (dunit) {
                const int ks = blockIdx.x >> 2, cgrp = blockIdx.x & 3, c8 = l & 15;
#pragma unroll
                for (int c = 0; c < 8; ++c) {
                    u32x4 t_;
#pragma unroll
                    for (int jj = 0; jj < 4; ++jj) {
                        const unsigned a = rr[0][2 * jj][c >> 1], b = rr[0][2 * jj + 1][c >> 1];
                        t_[jj] = (c & 1) ? ((a >> 16) | (b & 0xffff0000u)) : ((a & 0xffffu) | (b << 16));
                    }
                    const int n = c8 * 8 + c, cc = n & 31;
                    sm[(2 * (n >> 5) + ((cc >> 2) & 1)) * 65 + g * 16 + 4 * (cc >> 3) + (cc & 3)] = t_;
                }
                asm volatile("s_waitcnt lgkmcnt(0)" ::: "memory");
                __builtin_amdgcn_wave_barrier();
#pragma unroll
                for (int nf = 0; nf < 8; ++nf) bst(rs, (unsigned)(OFF_WD + (((size_t)(cgrp * 8 + nf) * KS_F + ks) * 64 + l) * 16), sm[nf * 65 + l]);
                DRAIN();
                if (l == 0) set_flag(cnt + (CNT_CD + cgrp) * 64 + ks);
            }
            if (wunit) {
                const int k0 = wkq * 4;
#pragma unroll
                for (int c = 0; c < 8; ++c) {
                    u32x4 t_;
#pragma unroll
                    for (int jj = 0; jj < 4; ++jj) {
                        const unsigned a = rr[0][2 * jj][c >> 1], b = rr[0][2 * jj + 1][c >> 1];
                        t_[jj] = (c & 1) ? ((a >> 16) | (b & 0xffff0000u)) : ((a & 0xffffu) | (b << 16));
                    }
                    const int n = (l & 3) * 8 + c;
                    sm[((l >> 4) * 2 + (n >> 4)) * 65 + ((l >> 2) & 3) * 16 + (n & 15)] = t_;
                }
                asm volatile("s_waitcnt lgkmcnt(0)" ::: "memory");
                __builtin_amdgcn_wave_barrier();
                f32x4 c1[2], c2[2];
                c1[0] = zero; c1[1] = zero; c2[0] = zero; c2[1] = zero;
#pragma unroll
                for (int i = 0; i < 4; ++i)
#pragma unroll
                    for (int nf = 0; nf < 2; ++nf) {
                        const u32x4 v = sm[(i * 2 + nf) * 65 + l];
                        c2[nf] = MFMA32(v, wbet[i], c2[nf]);
                        const u32x4 o_ = scale8(v, wgam[i]);
                        c1[nf] = MFMA32(o_, ones, c1[nf]);
                        bst(rs, (unsigned)(OFF_WGU2 + (((size_t)((wcg * 2 + nf) * 2 + wmat) * 16 + k0 + i) * 64 + l) * 16), o_);
                    }
                if ((l & 15) < 4) {
                    const int nf = (l >> 1) & 1, which = l & 1;
                    const f32x4 cv = which ? (nf ? c2[1] : c2[0]) : (nf ? c1[1] : c1[0]);
                    bst(rs, (unsigned)(OFF_C12 + ((((size_t)(wcg * 2 + nf) * 4 + wkq) * 2 + wmat) * 2 + which) * 64 + g * 16), __builtin_bit_cast(u32x4, cv));
                }
                DRAIN();
                if (l == 0) set_flag(cnt + (CNT_CW + (wcg >> 2)) * 64 + ((wcg & 3) * 4 + wkq) * 2 + wmat);
            }
            if (wv == 0) { WO_DMA(0); WO_DMA(1); }
            if (wv == 1) { WO_DMA(2); }
            if (wv == 2) { WO_DMA(3); }
            DRAIN();
            if (wv == 3) {
                wait_cnt(cnt + (CNT_CW + ng) * 64, ng == 10 ? 24u : 32u); wait_cnt(cnt + (CNT_CD + (nt2 >> 1)) * 64, 43u);
                if (l == 0) sFlag[4] = 1u;
            } else { unsigned sp = 0; while (sFlag[4] == 0u) { __builtin_amdgcn_s_sleep(2); if (++sp > (1u << 22)) break; } }
            asm volatile("" ::: "memory");
#pragma unroll
            for (int i = 0; i < B3_K; ++i) K3_LOADB(i);
        } else {
            {
#pragma unroll
                for (int mf = 0; mf < 4; ++mf)
                    *(u32x2*)(sV + w * 2048 + (mf * 16 + (l & 15)) * 32 + g * 8) = (u32x2){pk_bf16(ac[mf][0], ac[mf][1]), pk_bf16(ac[mf][2], ac[mf][3])};
                asm volatile("s_waitcnt lgkmcnt(0)" ::: "memory");
                if (l == 0) sFlag[8 + w] = 1u;
            }
            SB();
#pragma unroll
            for (int i = 8; i < 16; ++i) bq[i] = WQ[i * 64];
            SB();
            f32x4 aq[4];
#pragma unroll
            for (int mf = 0; mf < 4; ++mf) aq[mf] = zero;
            K1_RD3(0); K1_RD3(1);
#pragma unroll
            for (int ks = 0; ks < 16; ++ks) {
                if (ks + 2 < 16) K1_RD3(ks + 2);
#pragma unroll
                for (int mf = 0; mf < 4; ++mf) aq[mf] = MFMA32(bq[ks], a3[ks % 3][mf], aq[mf]);
                SB();
            }
            if (l == 0) __hip_atomic_fetch_add((unsigned*)&sFlag[6], 1u, __ATOMIC_RELAXED, __HIP_MEMORY_SCOPE_WORKGROUP);
            SB();
            u32x4 o[4];
            softmax_pm<4>(aq, o);
            wait_cnt(cnt + (CNT_CK + h) * 64, 32u);
            {
                u32x4 sl[32], zl[4];
#pragma unroll
                for (int i = 0; i < 4; ++i) zl[i] = bld(rs, (unsigned)(OFF_ZSL + ((size_t)h * 256 + i * 64 + l) * 16));
#pragma unroll
                for (int i = 0; i < 32; ++i) sl[i] = bld(rs, (unsigned)(OFF_SLAB + (((size_t)h * 32 + i) * 64 + l) * 16));
                SB();
                float z[4];
#pragma unroll
                for (int j = 0; j < 4; ++j) z[j] = (u2f(zl[0][j]) + u2f(zl[1][j])) + (u2f(zl[2][j]) + u2f(zl[3][j]));
#pragma unroll
                for (int j = 0; j < 4; ++j) z[j] += __shfl_xor(z[j], 8);
                red2_add(z[0], z[1], z[0], z[1]); red2_add(z[2], z[3], z[2], z[3]);
                if (l < 8) ((u32x2*)sZ)[w * 8 + l] = (u32x2){pk_bf16(z[0], z[1]), pk_bf16(z[2], z[3])};
                float a[8];
#pragma unroll
                for (int j = 0; j < 4; ++j) { a[2 * j] = bf_lo(sl[0][j]); a[2 * j + 1] = bf_hi(sl[0][j]); }
#pragma unroll
                for (int i = 1; i < 32; ++i)
#pragma unroll
                    for (int j = 0; j < 4; ++j) { a[2 * j] += bf_lo(sl[i][j]); a[2 * j + 1] += bf_hi(sl[i][j]); }
                const u32x4 ka = (u32x4){pk_bf16(a[0], a[1]), pk_bf16(a[2], a[3]), pk_bf16(a[4], a[5]), pk_bf16(a[6], a[7])};
                asm volatile("s_waitcnt lgkmcnt(0)" ::: "memory");
                __builtin_amdgcn_wave_barrier();
                const u32x4 za = sZ[w * 4 + g];
                f32x4 num[4], den[4];
#pragma unroll
                for (int mf = 0; mf < 4; ++mf) { num[mf] = MFMA32(ka, o[mf], zero); den[mf] = MFMA32(za, o[mf], zero); }
#pragma unroll
                for (int mp = 0; mp < 2; ++mp) {
                    const float r0 = frcp(den[2 * mp][0] + 1e-6f), r1 = frcp(den[2 * mp + 1][0] + 1e-6f);
                    bst(rs, (unsigned)(OFF_PHIQ + (((size_t)(mt * 2 + mp) * 32 + h) * 64 + l) * 16),
                        (u32x4){pk_bf16(num[2 * mp][0] * r0, num[2 * mp][1] * r0), pk_bf16(num[2 * mp][2] * r0, num[2 * mp][3] * r0),
                                pk_bf16(num[2 * mp + 1][0] * r1, num[2 * mp + 1][1] * r1), pk_bf16(num[2 * mp + 1][2] * r1, num[2 * mp + 1][3] * r1)});
                }
            }
            DRAIN();
            if (l == 0) {
                const unsigned old = __hip_atomic_fetch_add((unsigned*)&sFlag[2], 1u, __ATOMIC_RELAXED, __HIP_MEMORY_SCOPE_WORKGROUP);
                if ((old & 15u) == 3u) set_flag(cnt + (CNT_CP + mt) * 64 + hg);
            }
            if (wv != 7) {
                { unsigned sp = 0; while (sFlag[4] == 0u) { __builtin_amdgcn_s_sleep(2); if (++sp > (1u << 22)) break; } }
                asm volatile("" ::: "memory");
#pragma unroll
                for (int i = 0; i < B3_K; ++i) K3_LOADB(i);
            }
        }
    }
    if (wv == 7) { wait_cnt(cnt + (CNT_CP + mt2) * 64, 8u); if (l == 0) sFlag[0] = 1u; }
    else { unsigned sp = 0; while (sFlag[0] == 0u) { __builtin_amdgcn_s_sleep(1); if (++sp > (1u << 22)) break; } }
    asm volatile("" ::: "memory");
    u32x4 of[4][2];
#pragma unroll
    for (int hp = 0; hp < 4; ++hp) {
        const unsigned o_ = (unsigned)(OFF_PHIQ + (((size_t)(mt2 * 2 + mh) * 32 + kq * 8 + 2 * hp) * 64 + l) * 16);
        of[hp][0] = bld(rs, o_); of[hp][1] = bld(rs, o_ + 1024);
    }
    SB();
    const int fmf = 2 * mh + (kq >> 1), fp = kq & 1;
    const int tok2 = (mt2 * 4 + fmf) * 16 + (l & 15);
    const int n02 = nt2 * 64 + fp * 32 + g * 8;
    const u32x4 xv2 = *(const u32x4*)(x + (size_t)tok2 * D_ + n02);
    const u32x4 gv2 = *(const u32x4*)((const u16*)(ws + OFF_MOD) + 1024 + n02);
    SB();
    __syncthreads();
    {
        f32x4 acc[4][2];
#pragma unroll
        for (int a = 0; a < 4; ++a) { acc[a][0] = zero; acc[a][1] = zero; }
#pragma unroll
        for (int hp = 0; hp < 4; ++hp) {
#pragma unroll
            for (int i = hp * B3_PER; i < (hp + 1) * B3_PER; ++i) K3_LOADB(i);
            const u32x4 o0 = {of[hp][0][0], of[hp][0][1], of[hp][1][0], of[hp][1][1]}, o1 = {of[hp][0][2], of[hp][0][3], of[hp][1][2], of[hp][1][3]};
#pragma unroll
            for (int nf = 0; nf < 4; ++nf) {
                const u32x4 wf = sWO[(nf * 16 + kq * 4 + hp) * 64 + l];
                acc[nf][0] = MFMA32(wf, o0, acc[nf][0]); acc[nf][1] = MFMA32(wf, o1, acc[nf][1]);
            }
        }
        f32x4* sR = (f32x4*)smem;
#pragma unroll
        for (int nf = 0; nf < 4; ++nf)
#pragma unroll
            for (int mf = 0; mf < 2; ++mf) sR[(wv * 8 + nf * 2 + mf) * 64 + l] = acc[nf][mf];
        __syncthreads();
        f32x4 a0 = zero, a1 = zero;
#pragma unroll
        for (int ww = 0; ww < 4; ++ww) {
            a0 += sR[((mh * 4 + ww) * 8 + (2 * fp) * 2 + (kq >> 1)) * 64 + l];
            a1 += sR[((mh * 4 + ww) * 8 + (2 * fp + 1) * 2 + (kq >> 1)) * 64 + l];
        }
        u32x4 o;
        o[0] = pk_bf16(bf_lo(xv2[0]) + bf_lo(gv2[0]) * a0[0], bf_hi(xv2[0]) + bf_hi(gv2[0]) * a0[1]);
        o[1] = pk_bf16(bf_lo(xv2[1]) + bf_lo(gv2[1]) * a0[2], bf_hi(xv2[1]) + bf_hi(gv2[1]) * a0[3]);
        o[2] = pk_bf16(bf_lo(xv2[2]) + bf_lo(gv2[2]) * a1[0], bf_hi(xv2[2]) + bf_hi(gv2[2]) * a1[1]);
        o[3] = pk_bf16(bf_lo(xv2[3]) + bf_lo(gv2[3]) * a1[2], bf_hi(xv2[3]) + bf_hi(gv2[3]) * a1[3]);
        bst(rs, (unsigned)(OFF_X1 + (((size_t)(mt2 * 4 + fmf) * 16 + (nt2 * 2 + fp)) * 64 + l) * 16), o);
        float vals[8];
#pragma unroll
        for (int i = 0; i < 4; ++i) { vals[2 * i] = bf_lo(o[i]); vals[2 * i + 1] = bf_hi(o[i]); }
        float s = ((vals[0] + vals[1]) + (vals[2] + vals[3])) + ((vals[4] + vals[5]) + (vals[6] + vals[7])), m, dm;
        red2_add(s, 0.f, m, dm);
        m *= (1.f / 32.f);
        float q = 0.f, Q;
#pragma unroll
        for (int i = 0; i < 8; ++i) { const float d = vals[i] - m; q += d * d; }
        red2_add(q, 0.f, Q, dm);
        if (g == 0) bst64(rs, (unsigned)(OFF_ST + (((size_t)(mt2 * 4 + fmf) * 16 + nt2 * 2 + fp) * 16 + (l & 15)) * 8), (u32x2){f2u(m), f2u(Q)});
        DRAIN();
        __syncthreads();
        if (threadIdx.x == 0) set_flag(cnt + (CNT_CX + mt2) * 64 + nt2);
    }
    if (wv == 7) {
#pragma unroll
        for (int i = 0; i < B3_K; ++i) K3_LOADB(i);
    }
#pragma unroll
    for (int i = B3_K; i < B3_A; ++i) K3_LOADB(i);
    const unsigned wdoff = (unsigned)(OFF_WD + (((size_t)nt2 * 4 * KS_F + kq) * 64 + l) * 16);
    u32x4 wdf[11][4];
    const int last4 = kq < 3 ? 40 : 36;
#define K4_LOADW(j) do { const int i_ = (j) >> 2, f_ = (j) & 3; wdf[i_][f_] = bld(rs, wdoff + (f_ * KS_F + (i_ < 10 ? 4 * i_ : last4)) * 1024); } while (0)
    if (act3) {
        u32x4* sA = smem;
        if (wv == 6) {
            const int ma = (mf0 * 16) >> 6, mb = (mf0 * 16 + nmf * 16 - 1) >> 6;
            wait_cnt(cnt + (CNT_CX + ma) * 64, 8u);
            wait_cnt(cnt + (CNT_CX + mb) * 64, 8u);
        }
        __syncthreads();
        u32x4 xa[4][3];
        u32x2 stp[4];
#define K3_LDA(c) do { _Pragma("unroll") for (int i = 0; i < 3; ++i) { const int mf = 3 * mh + i, mfa = mf0 + (mf < nmf ? mf : 0); \
                xa[c][i] = bld(rs, (unsigned)(OFF_X1 + (((size_t)mfa * 16 + 4 * (c) + kq) * 64 + l) * 16)); } SB(); } while (0)
#define K3_WRA(c) do { _Pragma("unroll") for (int i = 0; i < 3; ++i) sA[((3 * mh + i) * 16 + 4 * (c) + kq) * 64 + l] = xa[c][i]; SB(); } while (0)
#define K3_RDA(buf, ks) do { _Pragma("unroll") for (int mf = 0; mf < 6; ++mf) af[buf][mf] = sA[(mf * 16 + (ks)) * 64 + l]; } while (0)
        K3_LDA(0); K3_LDA(1); K3_LDA(2); K3_LDA(3);
#pragma unroll
        for (int i = B3_A; i < 16; ++i) K3_LOADB(i);
        K3_WRA(0); K3_WRA(1); K3_WRA(2); K3_WRA(3);
        __syncthreads(); SB();
        f32x4 ag[6], au[6];
#pragma unroll
        for (int mf = 0; mf < 6; ++mf) { ag[mf] = zero; au[mf] = zero; }
        u32x4 af[2][6];
        f32x4 cpart;
        K3_RDA(0, 0);
#pragma unroll
        for (int ks = 0; ks < 16; ++ks) {
            if (ks == 2) {
                const int i = wv < 6 ? wv : 0, mfa = mf0 + (i < nmf ? i : 0);
#pragma unroll
                for (int j = 0; j < 4; ++j) stp[j] = bld64(rs, (unsigned)(OFF_ST + (((size_t)mfa * 16 + 4 * g + j) * 16 + (l & 15)) * 8));
                SB();
            }
            if (ks == 9) {
                float m, dummy, q;
                const float m0 = u2f(stp[0][0]), m1 = u2f(stp[1][0]), m2 = u2f(stp[2][0]), m3 = u2f(stp[3][0]);
                red2_add((m0 + m1) + (m2 + m3), 0.f, m, dummy);
                m *= 0.0625f;
                const float e0 = m0 - m, e1 = m1 - m, e2 = m2 - m, e3 = m3 - m;
                red2_add(((u2f(stp[0][1]) + u2f(stp[1][1])) + (u2f(stp[2][1]) + u2f(stp[3][1]))) + 32.f * ((e0 * e0 + e1 * e1) + (e2 * e2 + e3 * e3)), 0.f, q, dummy);
                if (g == 0 && wv < 6) sSt[wv * 16 + l] = (f32x2){rsqrtf(q * (1.f / D_) + 1e-5f), -m};
                SB();
            }
            if (ks == 11) { __syncthreads(); SB(); }
            if (ks == 12) {
                const unsigned co = (unsigned)(OFF_C12 + (size_t)(actw ? pair : ng * 8) * 1024 + (l & 15) * 64 + g * 16);
                cpart = __builtin_bit_cast(f32x4, bld(rs, co)); SB();
            }
            if (ks + 1 < 16) K3_RDA((ks + 1) & 1, ks + 1);
            SB();
#pragma unroll
            for (int mf = 0; mf < 6; ++mf) { ag[mf] = MFMA32(bg[ks], af[ks & 1][mf], ag[mf]); au[mf] = MFMA32(bu[ks], af[ks & 1][mf], au[mf]); }
            SB();
            if (ks >= 3 && ks < 15) { K4_LOADW(2 * (ks - 3)); K4_LOADW(2 * (ks - 3) + 1); SB(); }
        }
        f32x4 c1g, c2g, c1u, c2u;
#pragma unroll
        for (int j = 0; j < 4; ++j) {
            float v = cpart[j];
            v += u2f(__builtin_amdgcn_update_dpp(0, f2u(v), 0x124, 0xf, 0xf, false));
            v += u2f(__builtin_amdgcn_update_dpp(0, f2u(v), 0x128, 0xf, 0xf, false));
            c1g[j] = u2f(__builtin_amdgcn_update_dpp(0, f2u(v), 0x00, 0xf, 0xf, false));
            c2g[j] = u2f(__builtin_amdgcn_update_dpp(0, f2u(v), 0x55, 0xf, 0xf, false));
            c1u[j] = u2f(__builtin_amdgcn_update_dpp(0, f2u(v), 0xaa, 0xf, 0xf, false));
            c2u[j] = u2f(__builtin_amdgcn_update_dpp(0, f2u(v), 0xff, 0xf, 0xf, false));
        }
        if (actw) {
#pragma unroll
            for (int mp = 0; mp < 3; ++mp) {
                if (2 * mp < nmf) {
                    u32x4 ov;
#pragma unroll
                    for (int q = 0; q < 2; ++q) {
                        const int mf = 2 * mp + q;
                        const f32x2 st = sSt[mf * 16 + (l & 15)];
                        float hv[4];
#pragma unroll
                        for (int j = 0; j < 4; ++j) {
                            const float gq = (ag[mf][j] + st[1] * c1g[j]) * st[0] + c2g[j];
                            const float uq = (au[mf][j] + st[1] * c1u[j]) * st[0] + c2u[j];
                            hv[j] = gelu_tanh(gq) * uq;
                        }
                        ov[2 * q] = pk_bf16(hv[0], hv[1]); ov[2 * q + 1] = pk_bf16(hv[2], hv[3]);
                    }
                    bst(rs, (unsigned)(OFF_HH + (((size_t)((mf0 >> 1) + mp) * NPAIR + pair) * 64 + l) * 16), ov);
                }
            }
        }
        DRAIN();
        __syncthreads();
        if (threadIdx.x == 0) set_flag(cnt + (CNT_CH + mt3) * 64 + ng);
    } else {
#pragma unroll
        for (int j = 0; j < 24; ++j) K4_LOADW(j);
    }
#pragma unroll
    for (int j = 24; j < 44; ++j) K4_LOADW(j);
    SB();
    {
        if (wv == 7) {
            const int ma = (mt2 * 64) / 96, mb = (mt2 * 64 + 63) / 96;
            wait_cnt(cnt + (CNT_CH + ma) * 64, 11u);
            wait_cnt(cnt + (CNT_CH + mb) * 64, 11u);
        }
        __syncthreads();
        f32x4 acc[4][2];
#pragma unroll
        for (int a = 0; a < 4; ++a) { acc[a][0] = zero; acc[a][1] = zero; }
        u32x4 hf[11][2];
#define K4_LOADH(i) do { const unsigned o_ = (unsigned)(OFF_HH + (((size_t)(mt2 * 2 + mh) * NPAIR + 2 * (kq + ((i) < 10 ? 4 * (i) : last4))) * 64 + l) * 16); \
            hf[i][0] = bld(rs, o_); hf[i][1] = bld(rs, o_ + 1024); SB(); } while (0)
#pragma unroll
        for (int i = 0; i < 3; ++i) K4_LOADH(i);
        const u32x4 xv = bld(rs, (unsigned)(OFF_X1 + (((size_t)(mt2 * 4 + fmf) * 16 + (nt2 * 2 + fp)) * 64 + l) * 16));
        const u32x4 gv = *(const u32x4*)((const u16*)(ws + OFF_MOD) + 2560 + n02);
        SB();
#pragma unroll
        for (int i = 0; i < 11; ++i) {
            if (i + 3 < 11) K4_LOADH(i + 3);
            if (i < 10 || kq < 3) {
                const u32x4 h0 = {hf[i][0][0], hf[i][0][1], hf[i][1][0], hf[i][1][1]}, h1 = {hf[i][0][2], hf[i][0][3], hf[i][1][2], hf[i][1][3]};
#pragma unroll
                for (int nf = 0; nf < 4; ++nf) { acc[nf][0] = MFMA32(wdf[i][nf], h0, acc[nf][0]); acc[nf][1] = MFMA32(wdf[i][nf], h1, acc[nf][1]); }
            }
            SB();
        }
        f32x4* sR = (f32x4*)smem;
#pragma unroll
        for (int nf = 0; nf < 4; ++nf)
#pragma unroll
            for (int mf = 0; mf < 2; ++mf) sR[(wv * 8 + nf * 2 + mf) * 64 + l] = acc[nf][mf];
        __syncthreads();
        f32x4 a0 = zero, a1 = zero;
#pragma unroll
        for (int ww = 0; ww < 4; ++ww) {
            a0 += sR[((mh * 4 + ww) * 8 + (2 * fp) * 2 + (kq >> 1)) * 64 + l];
            a1 += sR[((mh * 4 + ww) * 8 + (2 * fp + 1) * 2 + (kq >> 1)) * 64 + l];
        }
        u32x4 o;
        o[0] = pk_bf16(bf_lo(xv[0]) + bf_lo(gv[0]) * a0[0], bf_hi(xv[0]) + bf_hi(gv[0]) * a0[1]);
        o[1] = pk_bf16(bf_lo(xv[1]) + bf_lo(gv[1]) * a0[2], bf_hi(xv[1]) + bf_hi(gv[1]) * a0[3]);
        o[2] = pk_bf16(bf_lo(xv[2]) + bf_lo(gv[2]) * a1[0], bf_hi(xv[2]) + bf_hi(gv[2]) * a1[1]);
        o[3] = pk_bf16(bf_lo(xv[3]) + bf_lo(gv[3]) * a1[2], bf_hi(xv[3]) + bf_hi(gv[3]) * a1[3]);
        *(u32x4*)(out + (size_t)tok2 * D_ + n02) = o;
    }

}

extern "C" void kernel_launch(void* const* d_in, const int* in_sizes, int n_in,
                              void* d_out, int out_size, void* d_ws, size_t ws_size,
                              hipStream_t stream) {
    const u16* x = (const u16*)d_in[0];
    const u16* wqkv = (const u16*)d_in[1];
    const u16* wphi = (const u16*)d_in[2];
    const u16* wo = (const u16*)d_in[3];
    const u16* wg = (const u16*)d_in[5];
    const u16* wu = (const u16*)d_in[6];
    const u16* wd = (const u16*)d_in[7];
    const u16* cvec = (const u16*)d_in[8];
    const u16* wmod = (const u16*)d_in[9];
    char* ws = (char*)d_ws;
    k0_prep<<<K0_GRID, 256, 0, stream>>>(x, wqkv, wphi, cvec, wmod, wo, ws);
    kf_main<<<256, 512, 0, stream>>>(x, wg, wu, wd, ws, (u16*)d_out);
}
```

```cpp
#include <hip/hip_runtime.h>
#include <hip/hip_bf16.h>
#include <stdint.h>

typedef short bf16x8 __attribute__((ext_vector_type(8)));
typedef short bf16x4 __attribute__((ext_vector_type(4)));
typedef float f32x4 __attribute__((ext_vector_type(4)));
typedef float f32x2 __attribute__((ext_vector_type(2)));
typedef unsigned u32x4 __attribute__((ext_vector_type(4)));
typedef unsigned u32x2 __attribute__((ext_vector_type(2)));
typedef __bf16 bf16x2_t __attribute__((ext_vector_type(2)));
typedef unsigned short u16;

#define PT_ST 2
#define K3_LA 4
#define DI __device__ __forceinline__
#define MFMA32(a, b, c) __builtin_amdgcn_mfma_f32_16x16x32_bf16(__builtin_bit_cast(bf16x8, (a)), __builtin_bit_cast(bf16x8, (b)), (c), 0, 0, 0)
#define SB() __builtin_amdgcn_sched_barrier(0)

constexpr int S_ = 2048, D_ = 512, H_ = 32, F_ = 1376;
constexpr int NMF = S_ / 16;
constexpr int KS_D = D_ / 32;
constexpr int KS_F = F_ / 32;
constexpr int NPAIR = F_ / 16;
constexpr float LOG2E = 1.4426950408889634f;

constexpr size_t OFF_MOD  = 0;
constexpr size_t OFF_CNT  = 8192;
constexpr size_t OFF_KVF  = OFF_CNT + 49152;
constexpr size_t OFF_ZF   = OFF_KVF + 65536;
constexpr size_t OFF_SLAB = OFF_ZF + 4096;
constexpr size_t OFF_ZSL  = OFF_SLAB + (size_t)32 * 32 * 1024;
constexpr size_t OFF_XN   = OFF_ZSL + (size_t)32 * 32 * 128;
constexpr size_t OFF_WQKV = OFF_XN + (size_t)S_ * D_ * 2;
constexpr size_t OFF_WO   = OFF_WQKV + (size_t)96 * 16384;
constexpr size_t OFF_WGU  = OFF_WO + (size_t)32 * 16384;
constexpr size_t OFF_WD   = OFF_WGU + (size_t)88 * 2 * 16384;
constexpr size_t OFF_PHIQ = OFF_WD + (size_t)32 * KS_F * 1024;
constexpr size_t OFF_X1   = OFF_PHIQ + (size_t)NMF * 32 * 1024;
constexpr size_t OFF_ST   = OFF_X1 + (size_t)S_ * D_ * 2;
constexpr size_t OFF_C12  = OFF_ST + (size_t)S_ * 16 * 8;
constexpr size_t OFF_WGU2 = OFF_C12 + (size_t)88 * 1024;
constexpr size_t OFF_HH   = OFF_WGU2 + (size_t)88 * 2 * 16384;

DI unsigned pk_bf16(float a, float b) {
    f32x2 v = {a, b};
    bf16x2_t r = __builtin_convertvector(v, bf16x2_t);
    return __builtin_bit_cast(unsigned, r);
}
DI float bf_lo(unsigned u) { return __uint_as_float(u << 16); }
DI float bf_hi(unsigned u) { return __uint_as_float(u & 0xffff0000u); }
DI float bf_round(float a) { return bf_lo(pk_bf16(a, 0.f)); }
DI float fexp2(float x) { return __builtin_amdgcn_exp2f(x); }
DI float frcp(float x) { return __builtin_amdgcn_rcpf(x); }

template <int KMAP, int NMAP>
DI void pretile_wave(const u16* __restrict__ W, int ldw, int ncols, int ks, int cgrp, u32x4* __restrict__ dst, int nks, int nf_mul, int nf_add,
                     u32x4* sm  ) {
    const int l = threadIdx.x & 63, g = l >> 4, c8 = l & 15;
    const int col0 = cgrp * 128 + c8 * 8;
    const bool ok = col0 < ncols;
    const int colc = ok ? col0 : 0;
    u32x4 r[8];
#pragma unroll
    for (int j = 0; j < 8; ++j) {
        const int k = ks * 32 + (KMAP == 0 ? 8 * g + j : 16 * (j >> 2) + 4 * g + (j & 3));
        r[j] = *(const u32x4*)(W + (size_t)k * ldw + colc);
    }
#pragma unroll
    for (int c = 0; c < 8; ++c) {
        u32x4 o;
#pragma unroll
        for (int jj = 0; jj < 4; ++jj) {
            const unsigned a = r[2 * jj][c >> 1], b = r[2 * jj + 1][c >> 1];
            o[jj] = (c & 1) ? ((a >> 16) | (b & 0xffff0000u)) : ((a & 0xffffu) | (b << 16));
        }
        const int n = c8 * 8 + c;
        int nf, rr;
        if (NMAP == 0) { nf = n >> 4; rr = n & 15; }
        else { const int cc = n & 31; nf = 2 * (n >> 5) + ((cc >> 2) & 1); rr = 4 * (cc >> 3) + (cc & 3); }
        sm[nf * 65 + g * 16 + rr] = o;
    }
    asm volatile("s_waitcnt lgkmcnt(0)" ::: "memory");
    __builtin_amdgcn_wave_barrier();
#pragma unroll
    for (int nf = 0; nf < 8; ++nf) {
        const int nfg = cgrp * 8 + nf;
        if (nfg * 16 < ncols) {
            const u32x4 o = sm[nf * 65 + l];
            u32x4* p_ = &dst[((size_t)(nfg * nf_mul + nf_add) * nks + ks) * 64 + l];
            asm volatile("global_store_dwordx4 %0, %1, off sc1\n\ts_nop 1" :: "v"(p_), "v"(o) : "memory");
        }
    }
}

DI float u2f(unsigned u) { return __uint_as_float(u); }
DI unsigned f2u(float f) { return __float_as_uint(f); }
DI void red2_max(float a, float b, float& A, float& B) {
    auto r = __builtin_amdgcn_permlane32_swap(f2u(a), f2u(b), false, false);
    const float c = fmaxf(u2f(r[0]), u2f(r[1]));
    auto r2 = __builtin_amdgcn_permlane16_swap(f2u(c), f2u(c), false, false);
    const float d = fmaxf(u2f(r2[0]), u2f(r2[1]));
    auto r3 = __builtin_amdgcn_permlane32_swap(f2u(d), f2u(d), false, false);
    A = u2f(r3[0]); B = u2f(r3[1]);
}
DI void red2_add(float a, float b, float& A, float& B) {
    auto r = __builtin_amdgcn_permlane32_swap(f2u(a), f2u(b), false, false);
    const float c = u2f(r[0]) + u2f(r[1]);
    auto r2 = __builtin_amdgcn_permlane16_swap(f2u(c), f2u(c), false, false);
    const float d = u2f(r2[0]) + u2f(r2[1]);
    auto r3 = __builtin_amdgcn_permlane32_swap(f2u(d), f2u(d), false, false);
    A = u2f(r3[0]); B = u2f(r3[1]);
}

constexpr int K0_MOD = 96, K0_XN = 128, K0_FOLD = 64, K0_WV = 16, K0_WO = 16, K0_WG = 0, K0_WU = 0, K0_WD = 0;
constexpr int K0_B0 = K0_MOD, K0_B1 = K0_B0 + K0_XN, K0_B2 = K0_B1 + K0_FOLD, K0_B3 = K0_B2 + K0_WV;
constexpr int K0_B4 = K0_B3 + K0_WO, K0_B5 = K0_B4 + K0_WG, K0_B6 = K0_B5 + K0_WU, K0_B7 = K0_B6 + K0_WD;
constexpr int K0_GRID = K0_B7;

__global__ __launch_bounds__(256) void k0_prep(const u16* __restrict__ x, const u16* __restrict__ wqkv, const u16* __restrict__ wphi,
                                               const u16* __restrict__ cvec, const u16* __restrict__ wmod,
                                               const u16* __restrict__ wo, char* __restrict__ ws) {
    __shared__ __attribute__((aligned(16))) u32x4 smT[4 * 520];
    float* red = (float*)smT;
    const int pb = blockIdx.x, t = threadIdx.x;
    const int b = pb < 160 ? pb + 96 : (pb < 256 ? pb - 160 : pb);
    if (b >= K0_B0 && b < K0_B0 + 12) ((u32x4*)(ws + OFF_CNT))[(b - K0_B0) * 256 + t] = (u32x4){0u, 0u, 0u, 0u};
    if (b < K0_B0) {
        const int cb = (b & 7) * 12 + (b >> 3);
        const int cgp = t & 3, kg = t >> 2;
        float acc[8];
#pragma unroll
        for (int i = 0; i < 8; ++i) acc[i] = 0.f;
        u32x4 wv[8]; float sc[8];
        const unsigned c2 = ((const unsigned*)cvec)[t];
#pragma unroll
        for (int i = 0; i < 8; ++i) wv[i] = *(const u32x4*)(wmod + (size_t)(kg + 64 * i) * 3072 + cb * 32 + cgp * 8);
        SB();
        {
            const float c0 = bf_lo(c2), c1 = bf_hi(c2);
            *(f32x2*)(red + 256 + 2 * t) = (f32x2){bf_round(c0 / (1.f + expf(-c0))), bf_round(c1 / (1.f + expf(-c1)))};
        }
        __syncthreads();
#pragma unroll
        for (int i = 0; i < 8; ++i) sc[i] = red[256 + kg + 64 * i];
#pragma unroll
        for (int i = 0; i < 8; ++i) {
#pragma unroll
            for (int j = 0; j < 4; ++j) {
                acc[2 * j] += sc[i] * bf_lo(wv[i][j]);
                acc[2 * j + 1] += sc[i] * bf_hi(wv[i][j]);
            }
        }
#pragma unroll
        for (int j = 0; j < 8; ++j) {
            acc[j] += u2f(__builtin_amdgcn_update_dpp(0, f2u(acc[j]), 0x124, 0xf, 0xf, false));
            acc[j] += u2f(__builtin_amdgcn_update_dpp(0, f2u(acc[j]), 0x128, 0xf, 0xf, false));
        }
#pragma unroll
        for (int j = 0; j < 8; j += 2) red2_add(acc[j], acc[j + 1], acc[j], acc[j + 1]);
        if ((t & 63) < 4) {
#pragma unroll
            for (int j = 0; j < 8; ++j) red[(t >> 6) * 32 + cgp * 8 + j] = acc[j];
        }
        __syncthreads();
        if (t < 32) {
            float s = bf_round((red[t] + red[32 + t]) + (red[64 + t] + red[96 + t]));
            const int n = cb * 32 + t, row = n >> 9;
            if (row == 1 || row == 4) s = 1.f + s;
            ((u16*)(ws + OFF_MOD))[n] = (u16)(pk_bf16(s, 0.f) & 0xffffu);
        }
    } else if (b < K0_B1) {
        const int mf = b - K0_B0, r = t & 15, c = t >> 4;
        const u16* xr = x + (size_t)(mf * 16 + r) * D_;
        u32x4 v[4];
#pragma unroll
        for (int i = 0; i < 4; ++i) v[i] = *(const u32x4*)(xr + (c + 16 * i) * 8);
        float f[32];
        float s = 0.f;
#pragma unroll
        for (int i = 0; i < 4; ++i)
#pragma unroll
            for (int j = 0; j < 4; ++j) { f[i * 8 + 2 * j] = bf_lo(v[i][j]); f[i * 8 + 2 * j + 1] = bf_hi(v[i][j]); s += f[i * 8 + 2 * j] + f[i * 8 + 2 * j + 1]; }
        s += __shfl_xor(s, 16); s += __shfl_xor(s, 32);
        if ((t & 63) < 16) red[(t >> 6) * 16 + r] = s;
        __syncthreads();
        const float m = ((red[r] + red[16 + r]) + (red[32 + r] + red[48 + r])) * (1.f / D_);
        float q = 0.f;
#pragma unroll
        for (int i = 0; i < 32; ++i) { f[i] -= m; q += f[i] * f[i]; }
        q += __shfl_xor(q, 16); q += __shfl_xor(q, 32);
        if ((t & 63) < 16) red[64 + (t >> 6) * 16 + r] = q;
        __syncthreads();
        const float var = (red[64 + r] + red[80 + r]) + (red[96 + r] + red[112 + r]);
        const float rs = rsqrtf(var * (1.f / D_) + 1e-5f);
        u32x4* dst = (u32x4*)(ws + OFF_XN) + (size_t)mf * 16 * 64;
#pragma unroll
        for (int i = 0; i < 4; ++i) {
            u32x4 o;
#pragma unroll
            for (int j = 0; j < 4; ++j) o[j] = pk_bf16(f[i * 8 + 2 * j] * rs, f[i * 8 + 2 * j + 1] * rs);
            const int ch = c + 16 * i;
            dst[(ch >> 2) * 64 + (ch & 3) * 16 + r] = o;
        }
    } else if (b < K0_B2) {
        const int wv = (b - K0_B1) * 4 + (t >> 6);
        const int hq = wv >> 2, kq = wv & 3;
        const int l = t & 63, r = l & 15, g = l >> 4, h = hq & 31;
        bf16x4 bphi;
#pragma unroll
        for (int j = 0; j < 4; ++j) bphi[j] = (short)wphi[h * 256 + (4 * g + j) * 16 + r];
        char* dst = ws + OFF_WQKV + (size_t)hq * 16384;
        bf16x4 a[8];
#pragma unroll
        for (int i = 0; i < 8; ++i) a[i] = *(const bf16x4*)(wqkv + (size_t)((kq * 8 + i) * 16 + r) * 1536 + hq * 16 + 4 * g);
#pragma unroll
        for (int i = 0; i < 8; ++i) {
            const int rb = kq * 8 + i;
            f32x4 d = {0.f, 0.f, 0.f, 0.f};
            d = __builtin_amdgcn_mfma_f32_16x16x16bf16_1k(a[i], bphi, d, 0, 0, 0);
            u32x2 o;
            o[0] = pk_bf16(d[0] * LOG2E, d[1] * LOG2E);
            o[1] = pk_bf16(d[2] * LOG2E, d[3] * LOG2E);
            const int ks = rb >> 1, gp = 2 * (rb & 1) + (g >> 1);
            *(u32x2*)(dst + ks * 1024 + (gp * 16 + r) * 16 + (g & 1) * 8) = o;
        }
    } else {
        u32x4* sm = smT + (t >> 6) * 520;
        if (b < K0_B3) {
            const int id = (b - K0_B2) * 4 + (t >> 6);
            pretile_wave<0, 0>(wqkv + 1024, 1536, 512, id >> 2, id & 3, (u32x4*)(ws + OFF_WQKV), KS_D, 1, 64, sm);
        } else {
            const int id = (b - K0_B3) * 4 + (t >> 6);
            pretile_wave<1, 1>(wo, 512, 512, id >> 2, id & 3, (u32x4*)(ws + OFF_WO), KS_D, 1, 0, sm);
        }
    }
}

DI float fmax2(float a, float b) { return __builtin_amdgcn_fmed3f(a, b, __builtin_inff()); }
DI float fmin2(float a, float b) { return __builtin_amdgcn_fmed3f(a, b, -__builtin_inff()); }
template <int N>
DI void softmax_pm(const f32x4 (&p)[N], u32x4 (&o)[N]) {
    float a[N], b[N];
#pragma unroll
    for (int i = 0; i < N; ++i) {
        a[i] = fmax2(fmax2(p[i][0], p[i][1]), fmax2(p[i][2], p[i][3]));
        b[i] = -fmin2(fmin2(p[i][0], p[i][1]), fmin2(p[i][2], p[i][3]));
    }
#pragma unroll
    for (int i = 0; i < N; ++i) { auto r = __builtin_amdgcn_permlane32_swap(f2u(a[i]), f2u(b[i]), false, false); a[i] = fmax2(u2f(r[0]), u2f(r[1])); }
#pragma unroll
    for (int i = 0; i < N; ++i) { auto r = __builtin_amdgcn_permlane16_swap(f2u(a[i]), f2u(a[i]), false, false); a[i] = fmax2(u2f(r[0]), u2f(r[1])); }
#pragma unroll
    for (int i = 0; i < N; ++i) { auto r = __builtin_amdgcn_permlane32_swap(f2u(a[i]), f2u(a[i]), false, false); a[i] = u2f(r[0]); b[i] = u2f(r[1]); }
    float ep[N][4], en[N][4];
#pragma unroll
    for (int i = 0; i < N; ++i)
#pragma unroll
        for (int j = 0; j < 4; ++j) { ep[i][j] = fexp2(p[i][j] - a[i]); en[i][j] = fexp2(-b[i] - p[i][j]); }
#pragma unroll
    for (int i = 0; i < N; ++i) { a[i] = (ep[i][0] + ep[i][1]) + (ep[i][2] + ep[i][3]); b[i] = (en[i][0] + en[i][1]) + (en[i][2] + en[i][3]); }
#pragma unroll
    for (int i = 0; i < N; ++i) { auto r = __builtin_amdgcn_permlane32_swap(f2u(a[i]), f2u(b[i]), false, false); a[i] = u2f(r[0]) + u2f(r[1]); }
#pragma unroll
    for (int i = 0; i < N; ++i) { auto r = __builtin_amdgcn_permlane16_swap(f2u(a[i]), f2u(a[i]), false, false); a[i] = frcp(u2f(r[0]) + u2f(r[1])); }
#pragma unroll
    for (int i = 0; i < N; ++i) { auto r = __builtin_amdgcn_permlane32_swap(f2u(a[i]), f2u(a[i]), false, false); a[i] = u2f(r[0]); b[i] = u2f(r[1]); }
#pragma unroll
    for (int i = 0; i < N; ++i) {
        o[i][0] = pk_bf16(ep[i][0] * a[i], ep[i][1] * a[i]); o[i][1] = pk_bf16(ep[i][2] * a[i], ep[i][3] * a[i]);
        o[i][2] = pk_bf16(en[i][0] * b[i], en[i][1] * b[i]); o[i][3] = pk_bf16(en[i][2] * b[i], en[i][3] * b[i]);
    }
}

DI u32x4 modulate8(const u32x4 xv, const float (&gg)[8], const float (&ss)[8]) {
    u32x4 o;
#pragma unroll
    for (int j = 0; j < 4; ++j)
        o[j] = pk_bf16(bf_lo(xv[j]) * gg[2 * j] + ss[2 * j], bf_hi(xv[j]) * gg[2 * j + 1] + ss[2 * j + 1]);
    return o;
}

#define RLX_AGENT __ATOMIC_RELAXED, __HIP_MEMORY_SCOPE_AGENT
constexpr int CNT_CK = 0, CNT_CV = 32, CNT_CP = 64, CNT_CX = 96, CNT_CH = 128, CNT_CW = 150, CNT_CT = 170, CNT_CD = 172;
typedef __amdgpu_buffer_rsrc_t rsrc_t;
DI u32x4 bld(rsrc_t rs, unsigned off) { return __builtin_amdgcn_raw_buffer_load_b128(rs, off, 0, 16); }
DI u32x2 bld64(rsrc_t rs, unsigned off) { return __builtin_amdgcn_raw_buffer_load_b64(rs, off, 0, 16); }
DI void bst(rsrc_t rs, unsigned off, u32x4 v) { __builtin_amdgcn_raw_buffer_store_b128(v, rs, off, 0, 16); }
DI void bst64(rsrc_t rs, unsigned off, u32x2 v) { __builtin_amdgcn_raw_buffer_store_b64(v, rs, off, 0, 16); }
#define POLL_SLEEP 4
#define DRAIN() asm volatile("s_waitcnt vmcnt(0)" ::: "memory")
DI void wait_cnt(unsigned* c, unsigned n) {
    unsigned sp = 0;
    const unsigned lane = threadIdx.x & 63;
    unsigned* p = c + (lane < n ? lane : 0);
    while (!__all(__hip_atomic_load(p, RLX_AGENT) != 0u)) { __builtin_amdgcn_s_sleep(POLL_SLEEP); if (++sp > (1u << 18)) break; }
    __builtin_amdgcn_fence(__ATOMIC_ACQUIRE, "wavefront");
    asm volatile("" ::: "memory");
}
DI void set_flag(unsigned* c) { __hip_atomic_store(c, 1u, RLX_AGENT); }
DI float gelu_tanh(float g) {
    const float k0 = -2.f * 0.7978845608028654f * LOG2E, k1 = k0 * 0.044715f;
    const float a = g * (g * g * k1 + k0);
    return g * frcp(1.f + fexp2(a));
}
DI u32x4 scale8(const u32x4 v, const u32x4 gv) {
    u32x4 o;
#pragma unroll
    for (int j = 0; j < 4; ++j) o[j] = pk_bf16(bf_lo(v[j]) * bf_lo(gv[j]), bf_hi(v[j]) * bf_hi(gv[j]));
    return o;
}

#define B3_EARLY 10
#define B3_K 12
#define B3_A 16
#define B3_PER 0
#define B3_MID 12
extern "C" __global__ __launch_bounds__(512, 2) void kf_main(const u16* __restrict__ x, const u16* __restrict__ wg, const u16* __restrict__ wu, const u16* __restrict__ wd,
                                                                char* __restrict__ ws, u16* __restrict__ out) {
    __shared__ __attribute__((aligned(16))) u32x4 smem[9216 + 128 + 128 + 64 + 128 + 4];
    const int xcd = blockIdx.x & 7, bi = blockIdx.x >> 3;
    const int wv = __builtin_amdgcn_readfirstlane(threadIdx.x >> 6);
    const int l = threadIdx.x & 63, g = l >> 4;
    unsigned* cnt = (unsigned*)(ws + OFF_CNT);
    const rsrc_t rs = __builtin_amdgcn_make_buffer_rsrc(ws, 0, 0x4000000, 0x00020000);
    u32x4* sWO = smem + 5120;
    u32x4* sZ = smem + 9216;
    u32x4* sGB = smem + 9216 + 128;
    f32x2* sSt = (f32x2*)(smem + 9216 + 256);
    typedef __attribute__((address_space(3))) unsigned lds_u32;
    volatile lds_u32* sFlag = (volatile lds_u32*)(smem + 9216 + 448);
    const u32x4 ones = {0x3f803f80u, 0x3f803f80u, 0x3f803f80u, 0x3f803f80u};
    const f32x4 zero = {0.f, 0.f, 0.f, 0.f};

    const int mt2 = 4 * xcd + (bi >> 3), nt2 = bi & 7;
    const int kq = wv & 3, mh = wv >> 2;
    const int t0_ = (242 * xcd) >> 3, t1_ = (242 * (xcd + 1)) >> 3;
    const int id3 = t0_ + bi;
    const bool act3 = id3 < t1_;
    const int id3c = act3 ? id3 : t0_;
    const int ng = (id3c % 121) / 11, mt3 = 11 * (id3c / 121) + (id3c % 121) % 11;
    const int mf0 = mt3 * 6;
    const int nmf = (NMF - mf0) < 6 ? (NMF - mf0) : 6;
    const int pair = ng * 8 + wv;
    const bool actw = pair < NPAIR;
    const unsigned wgoff = (unsigned)(OFF_WGU2 + ((size_t)(actw ? pair : ng * 8) * 2048 + l) * 16);
    u32x4 bg[16], bu[16];
#define K3_LOADB(ks) do { bg[ks] = bld(rs, wgoff + (ks) * 1024); bu[ks] = bld(rs, wgoff + 16384 + (ks) * 1024); SB(); } while (0)

#define WO_DMA(part) do { const u32x4* WO_ = (const u32x4*)(ws + OFF_WO) + ((size_t)nt2 * 64 + (part) * 8) * 64 + l; \
        const unsigned ldsb_ = (unsigned)(size_t)(sWO + (part) * 8 * 64); \
        _Pragma("unroll") for (int i_ = 0; i_ < 8; ++i_) { unsigned keep_; \
            asm volatile("s_mov_b32 %0, m0\n\ts_mov_b32 m0, %2\n\ts_nop 0\n\tglobal_load_lds_dwordx4 %1, off\n\ts_mov_b32 m0, %0" : "=&s"(keep_) : "v"(WO_ + i_ * 64), "s"(ldsb_ + i_ * 1024) : "memory"); } } while (0)
    if (threadIdx.x < 16) sFlag[threadIdx.x] = 0u;
    {
        u32x4* sA = smem;
        char* sP = (char*)(smem + 4096);
        const int mt = 8 * (xcd >> 1) + (bi >> 2), hg = 4 * (xcd & 1) + (bi & 3);
        const int w = wv & 3;
        const int h = hg * 4 + w;
        const u32x4* WQ = (const u32x4*)(ws + OFF_WQKV) + (size_t)h * 1024 + l;
        u32x4 af[2][4];
#define K1_RDA(buf, ks) do { _Pragma("unroll") for (int mf = 0; mf < 4; ++mf) af[buf][mf] = sA[(mf * 16 + (ks)) * 64 + l]; } while (0)
        const u32x4* XN = (const u32x4*)(ws + OFF_XN) + (size_t)mt * 4096 + mh * 2048 + w * 64 + l;
        const u32x4* MS = (const u32x4*)(ws + OFF_MOD) + w * 4 + g;
        const u32x4* WB = WQ + (wv < 4 ? 32 : 64) * 1024;
        u32x4 xa[4][2], gs[4][2], bb[16];
#define K1_LOADA(c) do { gs[c][0] = MS[64 + (c) * 16]; gs[c][1] = MS[(c) * 16]; \
        _Pragma("unroll") for (int mf = 0; mf < 2; ++mf) xa[c][mf] = XN[mf * 1024 + (c) * 256]; SB(); } while (0)
#define K1_PREP1(c, mf) do { float gg[8], ss[8]; \
        _Pragma("unroll") for (int j = 0; j < 4; ++j) { gg[2 * j] = bf_lo(gs[c][0][j]); gg[2 * j + 1] = bf_hi(gs[c][0][j]); ss[2 * j] = bf_lo(gs[c][1][j]); ss[2 * j + 1] = bf_hi(gs[c][1][j]); } \
        sA[((2 * mh + (mf)) * 16 + 4 * (c) + w) * 64 + l] = modulate8(xa[c][mf], gg, ss); } while (0)
#define K1_LOADB(ks) do { bb[ks] = WB[(ks) * 64]; } while (0)
        f32x4 ac[4];
#pragma unroll
        for (int mf = 0; mf < 4; ++mf) ac[mf] = zero;
        K1_LOADA(0); K1_LOADA(1);
        K1_LOADB(0); K1_LOADB(1); K1_LOADB(2); K1_LOADB(3); SB();
        K1_LOADA(2); K1_LOADA(3);
        K1_LOADB(4); K1_LOADB(5); K1_LOADB(6); K1_LOADB(7); SB();
        K1_PREP1(0, 0); K1_PREP1(0, 1); K1_PREP1(1, 0); K1_PREP1(1, 1);
        __syncthreads(); SB();
        u32x4 bq[16];
        u32x4 a3[3][4];
#define K1_RD3(ks) do { _Pragma("unroll") for (int mf = 0; mf < 4; ++mf) a3[(ks) % 3][mf] = sA[(mf * 16 + (ks)) * 64 + l]; } while (0)
        K1_RD3(0); K1_RD3(1);
#pragma unroll
        for (int ks = 0; ks < 16; ++ks) {
            if (ks + 8 < 16) K1_LOADB(ks + 8);
            else if (wv >= 4) bq[ks - 8] = WQ[(ks - 8) * 64];
            if (ks == 6) { __syncthreads(); }
            if (ks + 2 < 16) K1_RD3(ks + 2);
#pragma unroll
            for (int mf = 0; mf < 4; ++mf) ac[mf] = MFMA32(bb[ks], a3[ks % 3][mf], ac[mf]);
            if (ks == 0) K1_PREP1(2, 0);
            if (ks == 1) K1_PREP1(2, 1);
            if (ks == 2) K1_PREP1(3, 0);
            if (ks == 3) K1_PREP1(3, 1);
            SB();
        }
        char* sV = (char*)(sWO + 3584);
        if (wv < 4) {
            __builtin_amdgcn_s_setprio(3);
            const bool dunit = (wv == 1 || wv == 2) && blockIdx.x < 84 || (wv == 0 && blockIdx.x < 4);
            const int dun = wv == 0 ? 168 + blockIdx.x : blockIdx.x * 2 + (wv - 1);
            const bool wunit = (wv == 1 || wv == 2) && blockIdx.x >= 84;
            const int wmat = wv - 1, wcg = ((int)blockIdx.x - 84) >> 2, wkq = blockIdx.x & 3;
            u32x4 rr[1][8], wgam[4], wbet[4];
            if (dunit) {
                const int ks = dun >> 2, cgrp = dun & 3;
#pragma unroll
                for (int j = 0; j < 8; ++j) rr[0][j] = *(const u32x4*)(wd + (size_t)(ks * 32 + 16 * (j >> 2) + 4 * g + (j & 3)) * 512 + cgrp * 128 + (l & 15) * 8);
            }
            if (wunit) {
                const u16* W = wmat ? wu : wg;
#pragma unroll
                for (int j = 0; j < 8; ++j)
                    rr[0][j] = *(const u32x4*)(W + (size_t)((wkq * 4 + (l >> 4)) * 32 + 8 * ((l >> 2) & 3) + j) * F_ + wcg * 32 + (l & 3) * 8);
#pragma unroll
                for (int i = 0; i < 4; ++i) {
                    wgam[i] = ((const u32x4*)(ws + OFF_MOD))[256 + (wkq * 4 + i) * 4 + g];
                    wbet[i] = ((const u32x4*)(ws + OFF_MOD))[192 + (wkq * 4 + i) * 4 + g];
                }
            }
            SB();
            char* myP = sP + w * 4096;
            {
                u32x4 o[4];
                softmax_pm<4>(ac, o);
#pragma unroll
                for (int mf = 0; mf < 4; ++mf) {
                    char* row = myP + (mf * 16 + (l & 15)) * 64;
                    *(u32x2*)(row + g * 8) = (u32x2){o[mf][0], o[mf][1]};
                    *(u32x2*)(row + 32 + g * 8) = (u32x2){o[mf][2], o[mf][3]};
                }
            }
            const unsigned base = (unsigned)(size_t)myP + (4 * g + ((l & 15) >> 2)) * 64 + (l & 3) * 8;
            u32x2 t0, t1, t2, t3, t4, t5, t6, t7;
            asm volatile("s_waitcnt lgkmcnt(0)\n\t"
                         "ds_read_b64_tr_b16 %0, %8\n\tds_read_b64_tr_b16 %1, %8 offset:1024\n\t"
                         "ds_read_b64_tr_b16 %2, %8 offset:32\n\tds_read_b64_tr_b16 %3, %8 offset:1056\n\t"
                         "ds_read_b64_tr_b16 %4, %8 offset:2048\n\tds_read_b64_tr_b16 %5, %8 offset:3072\n\t"
                         "ds_read_b64_tr_b16 %6, %8 offset:2080\n\tds_read_b64_tr_b16 %7, %8 offset:3104\n\t"
                         "s_waitcnt lgkmcnt(0)"
                         : "=&v"(t0), "=&v"(t1), "=&v"(t2), "=&v"(t3), "=&v"(t4), "=&v"(t5), "=&v"(t6), "=&v"(t7) : "v"(base) : "memory");
            if (l == 0) __hip_atomic_fetch_add((lds_u32*)&sFlag[2], 16u, __ATOMIC_RELAXED, __HIP_MEMORY_SCOPE_WORKGROUP);
            const u32x4 pa00 = {t0[0], t0[1], t1[0], t1[1]}, pa01 = {t2[0], t2[1], t3[0], t3[1]};
            const u32x4 pa10 = {t4[0], t4[1], t5[0], t5[1]}, pa11 = {t6[0], t6[1], t7[0], t7[1]};
            { unsigned sp = 0; while (sFlag[8 + w] == 0u) { __builtin_amdgcn_s_sleep(1); if (++sp > (1u << 22)) break; } }
            asm volatile("" ::: "memory");
            u32x4 vb0, vb1;
            {
                const unsigned vbase = (unsigned)(size_t)sV + w * 2048 + (4 * g + ((l & 15) >> 2)) * 32 + (l & 3) * 8;
                u32x2 v0, v1, v2, v3;
                asm volatile("ds_read_b64_tr_b16 %0, %4\n\tds_read_b64_tr_b16 %1, %4 offset:512\n\t"
                             "ds_read_b64_tr_b16 %2, %4 offset:1024\n\tds_read_b64_tr_b16 %3, %4 offset:1536\n\ts_waitcnt lgkmcnt(0)"
                             : "=&v"(v0), "=&v"(v1), "=&v"(v2), "=&v"(v3) : "v"(vbase) : "memory");
                vb0 = (u32x4){v0[0], v0[1], v1[0], v1[1]}; vb1 = (u32x4){v2[0], v2[1], v3[0], v3[1]};
            }
            f32x4 kv0 = MFMA32(pa00, vb0, zero), kv1 = MFMA32(pa01, vb0, zero);
            f32x4 z0 = MFMA32(pa00, ones, zero), z1 = MFMA32(pa01, ones, zero);
            kv0 = MFMA32(pa10, vb1, kv0); kv1 = MFMA32(pa11, vb1, kv1);
            z0 = MFMA32(pa10, ones, z0); z1 = MFMA32(pa11, ones, z1);
            u32x4 o;
            o[0] = pk_bf16(kv0[0], kv0[1]); o[1] = pk_bf16(kv0[2], kv0[3]); o[2] = pk_bf16(kv1[0], kv1[1]); o[3] = pk_bf16(kv1[2], kv1[3]);
            bst(rs, (unsigned)(OFF_SLAB + (((size_t)h * 32 + mt) * 64 + l) * 16), o);
            if ((l & 15) < 2) {
                const f32x4 zz = (l & 1) ? z1 : z0;
                bst(rs, (unsigned)(OFF_ZSL + (((size_t)h * 32 + mt) * 8 + g * 2 + (l & 1)) * 16), __builtin_bit_cast(u32x4, zz));
            }
            DRAIN();
            if (l == 0) set_flag(cnt + (CNT_CK + h) * 64 + mt);
            __builtin_amdgcn_s_setprio(0);
            if (l == 0) __hip_atomic_fetch_add((lds_u32*)&sFlag[5], 1u, __ATOMIC_RELAXED, __HIP_MEMORY_SCOPE_WORKGROUP);
            SB();
            if (wv == 3) {
                { unsigned sp = 0; while (sFlag[5] != 4u) { __builtin_amdgcn_s_sleep(1); if (++sp > (1u << 22)) break; } }
                WO_DMA(7); WO_DMA(6); WO_DMA(5); WO_DMA(4);
            }
            if (wv == 0) { WO_DMA(0); WO_DMA(1); WO_DMA(2); WO_DMA(3); }
            if (dunit || wunit) {
                { unsigned sp = 0; while (sFlag[6] != 4u || (sFlag[2] >> 4) != 4u) { __builtin_amdgcn_s_sleep(1); if (++sp > (1u << 22)) break; } }
                asm volatile("" ::: "memory");
            }

            u32x4* sm = smem + w * 1040;
            if (dunit) {
                const int ks = dun >> 2, cgrp = dun & 3, c8 = l & 15;
#pragma unroll
                for (int c = 0; c < 8; ++c) {
                    u32x4 t_;
#pragma unroll
                    for (int jj = 0; jj < 4; ++jj) {
                        const unsigned a = rr[0][2 * jj][c >> 1], b = rr[0][2 * jj + 1][c >> 1];
                        t_[jj] = (c & 1) ? ((a >> 16) | (b & 0xffff0000u)) : ((a & 0xffffu) | (b << 16));
                    }
                    const int n = c8 * 8 + c, cc = n & 31;
                    sm[(2 * (n >> 5) + ((cc >> 2) & 1)) * 65 + g * 16 + 4 * (cc >> 3) + (cc & 3)] = t_;
                }
                asm volatile("s_waitcnt lgkmcnt(0)" ::: "memory");
                __builtin_amdgcn_wave_barrier();
#pragma unroll
                for (int nf = 0; nf < 8; ++nf) bst(rs, (unsigned)(OFF_WD + (((size_t)(cgrp * 8 + nf) * KS_F + ks) * 64 + l) * 16), sm[nf * 65 + l]);
                DRAIN();
                if (l == 0) set_flag(cnt + (CNT_CD + cgrp) * 64 + ks);
            }
            if (wunit) {
                const int k0 = wkq * 4;
#pragma unroll
                for (int c = 0; c < 8; ++c) {
                    u32x4 t_;
#pragma unroll
                    for (int jj = 0; jj < 4; ++jj) {
                        const unsigned a = rr[0][2 * jj][c >> 1], b = rr[0][2 * jj + 1][c >> 1];
                        t_[jj] = (c & 1) ? ((a >> 16) | (b & 0xffff0000u)) : ((a & 0xffffu) | (b << 16));
                    }
                    const int n = (l & 3) * 8 + c;
                    sm[((l >> 4) * 2 + (n >> 4)) * 65 + ((l >> 2) & 3) * 16 + (n & 15)] = t_;
                }
                asm volatile("s_waitcnt lgkmcnt(0)" ::: "memory");
                __builtin_amdgcn_wave_barrier();
                f32x4 c1[2], c2[2];
                c1[0] = zero; c1[1] = zero; c2[0] = zero; c2[1] = zero;
#pragma unroll
                for (int i = 0; i < 4; ++i)
#pragma unroll
                    for (int nf = 0; nf < 2; ++nf) {
                        const u32x4 v = sm[(i * 2 + nf) * 65 + l];
                        c2[nf] = MFMA32(v, wbet[i], c2[nf]);
                        const u32x4 o_ = scale8(v, wgam[i]);
                        c1[nf] = MFMA32(o_, ones, c1[nf]);
                        bst(rs, (unsigned)(OFF_WGU2 + (((size_t)((wcg * 2 + nf) * 2 + wmat) * 16 + k0 + i) * 64 + l) * 16), o_);
                    }
                if ((l & 15) < 4) {
                    const int nf = (l >> 1) & 1, which = l & 1;
                    const f32x4 cv = which ? (nf ? c2[1] : c2[0]) : (nf ? c1[1] : c1[0]);
                    bst(rs, (unsigned)(OFF_C12 + ((((size_t)(wcg * 2 + nf) * 4 + wkq) * 2 + wmat) * 2 + which) * 64 + g * 16), __builtin_bit_cast(u32x4, cv));
                }
                DRAIN();
                if (l == 0) set_flag(cnt + (CNT_CW + (wcg >> 2)) * 64 + ((wcg & 3) * 4 + wkq) * 2 + wmat);
            }
            DRAIN();
            if (wv == 3) {
                wait_cnt(cnt + (CNT_CW + ng) * 64, ng == 10 ? 24u : 32u);
                if (l == 0) sFlag[4] = 1u;
                wait_cnt(cnt + (CNT_CD + (nt2 >> 1)) * 64, 43u);
            } else { unsigned sp = 0; while (sFlag[4] == 0u) { __builtin_amdgcn_s_sleep(2); if (++sp > (1u << 22)) break; } }
            { unsigned sp = 0; while ((sFlag[2] & 15u) != 4u) { __builtin_amdgcn_s_sleep(2); if (++sp > (1u << 22)) break; } }
            asm volatile("" ::: "memory");
#pragma unroll
            for (int i = 0; i < B3_K; ++i) K3_LOADB(i);
        } else {
            {
#pragma unroll
                for (int mf = 0; mf < 4; ++mf)
                    *(u32x2*)(sV + w * 2048 + (mf * 16 + (l & 15)) * 32 + g * 8) = (u32x2){pk_bf16(ac[mf][0], ac[mf][1]), pk_bf16(ac[mf][2], ac[mf][3])};
                asm volatile("s_waitcnt lgkmcnt(0)" ::: "memory");
                if (l == 0) sFlag[8 + w] = 1u;
            }
            SB();
#pragma unroll
            for (int i = 8; i < 16; ++i) bq[i] = WQ[i * 64];
            SB();
            f32x4 aq[4];
#pragma unroll
            for (int mf = 0; mf < 4; ++mf) aq[mf] = zero;
            K1_RD3(0); K1_RD3(1);
#pragma unroll
            for (int ks = 0; ks < 16; ++ks) {
                if (ks + 2 < 16) K1_RD3(ks + 2);
#pragma unroll
                for (int mf = 0; mf < 4; ++mf) aq[mf] = MFMA32(bq[ks], a3[ks % 3][mf], aq[mf]);
                SB();
            }
            if (l == 0) __hip_atomic_fetch_add((lds_u32*)&sFlag[6], 1u, __ATOMIC_RELAXED, __HIP_MEMORY_SCOPE_WORKGROUP);
            SB();
            u32x4 o[4];
            softmax_pm<4>(aq, o);
            wait_cnt(cnt + (CNT_CK + h) * 64, 32u);
            {
                u32x4 sl[32], zl[4];
#pragma unroll
                for (int i = 0; i < 4; ++i) zl[i] = bld(rs, (unsigned)(OFF_ZSL + ((size_t)h * 256 + i * 64 + l) * 16));
#pragma unroll
                for (int i = 0; i < 32; ++i) sl[i] = bld(rs, (unsigned)(OFF_SLAB + (((size_t)h * 32 + i) * 64 + l) * 16));
                SB();
                float z[4];
#pragma unroll
                for (int j = 0; j < 4; ++j) z[j] = (u2f(zl[0][j]) + u2f(zl[1][j])) + (u2f(zl[2][j]) + u2f(zl[3][j]));
#pragma unroll
                for (int j = 0; j < 4; ++j) z[j] += __shfl_xor(z[j], 8);
                red2_add(z[0], z[1], z[0], z[1]); red2_add(z[2], z[3], z[2], z[3]);
                if (l < 8) ((u32x2*)sZ)[w * 8 + l] = (u32x2){pk_bf16(z[0], z[1]), pk_bf16(z[2], z[3])};
                float a[8];
#pragma unroll
                for (int j = 0; j < 4; ++j) { a[2 * j] = bf_lo(sl[0][j]); a[2 * j + 1] = bf_hi(sl[0][j]); }
#pragma unroll
                for (int i = 1; i < 32; ++i)
#pragma unroll
                    for (int j = 0; j < 4; ++j) { a[2 * j] += bf_lo(sl[i][j]); a[2 * j + 1] += bf_hi(sl[i][j]); }
                const u32x4 ka = (u32x4){pk_bf16(a[0], a[1]), pk_bf16(a[2], a[3]), pk_bf16(a[4], a[5]), pk_bf16(a[6], a[7])};
                asm volatile("s_waitcnt lgkmcnt(0)" ::: "memory");
                __builtin_amdgcn_wave_barrier();
                const u32x4 za = sZ[w * 4 + g];
                f32x4 num[4], den[4];
#pragma unroll
                for (int mf = 0; mf < 4; ++mf) { num[mf] = MFMA32(ka, o[mf], zero); den[mf] = MFMA32(za, o[mf], zero); }
#pragma unroll
                for (int mp = 0; mp < 2; ++mp) {
                    const float r0 = frcp(den[2 * mp][0] + 1e-6f), r1 = frcp(den[2 * mp + 1][0] + 1e-6f);
                    bst(rs, (unsigned)(OFF_PHIQ + (((size_t)(mt * 2 + mp) * 32 + h) * 64 + l) * 16),
                        (u32x4){pk_bf16(num[2 * mp][0] * r0, num[2 * mp][1] * r0), pk_bf16(num[2 * mp][2] * r0, num[2 * mp][3] * r0),
                                pk_bf16(num[2 * mp + 1][0] * r1, num[2 * mp + 1][1] * r1), pk_bf16(num[2 * mp + 1][2] * r1, num[2 * mp + 1][3] * r1)});
                }
            }
            DRAIN();
            if (l == 0) {
                const unsigned old = __hip_atomic_fetch_add((lds_u32*)&sFlag[2], 1u, __ATOMIC_RELAXED, __HIP_MEMORY_SCOPE_WORKGROUP);
                if ((old & 15u) == 3u) set_flag(cnt + (CNT_CP + mt) * 64 + hg);
            }
            if (wv != 7) {
                { unsigned sp = 0; while (sFlag[4] == 0u) { __builtin_amdgcn_s_sleep(2); if (++sp > (1u << 22)) break; } }
                asm volatile("" ::: "memory");
#pragma unroll
                for (int i = 0; i < B3_K; ++i) K3_LOADB(i);
            }
        }
    }
    if (wv == 7) { wait_cnt(cnt + (CNT_CP + mt2) * 64, 8u); if (l == 0) sFlag[0] = 1u; }
    else { unsigned sp = 0; while (sFlag[0] == 0u) { __builtin_amdgcn_s_sleep(1); if (++sp > (1u << 22)) break; } }
    asm volatile("" ::: "memory");
    u32x4 of[4][2];
#pragma unroll
    for (int hp = 0; hp < 4; ++hp) {
        const unsigned o_ = (unsigned)(OFF_PHIQ + (((size_t)(mt2 * 2 + mh) * 32 + kq * 8 + 2 * hp) * 64 + l) * 16);
        of[hp][0] = bld(rs, o_); of[hp][1] = bld(rs, o_ + 1024);
    }
    SB();
    const int fmf = 2 * mh + (kq >> 1), fp = kq & 1;
    const int tok2 = (mt2 * 4 + fmf) * 16 + (l & 15);
    const int n02 = nt2 * 64 + fp * 32 + g * 8;
    const u32x4 xv2 = *(const u32x4*)(x + (size_t)tok2 * D_ + n02);
    const u32x4 gv2 = *(const u32x4*)((const u16*)(ws + OFF_MOD) + 1024 + n02);
    SB();
    __syncthreads();
    {
        f32x4 acc[4][2];
#pragma unroll
        for (int a = 0; a < 4; ++a) { acc[a][0] = zero; acc[a][1] = zero; }
#pragma unroll
        for (int hp = 0; hp < 4; ++hp) {
#pragma unroll
            for (int i = hp * B3_PER; i < (hp + 1) * B3_PER; ++i) K3_LOADB(i);
            const u32x4 o0 = {of[hp][0][0], of[hp][0][1], of[hp][1][0], of[hp][1][1]}, o1 = {of[hp][0][2], of[hp][0][3], of[hp][1][2], of[hp][1][3]};
#pragma unroll
            for (int nf = 0; nf < 4; ++nf) {
                const u32x4 wf = sWO[(nf * 16 + kq * 4 + hp) * 64 + l];
                acc[nf][0] = MFMA32(wf, o0, acc[nf][0]); acc[nf][1] = MFMA32(wf, o1, acc[nf][1]);
            }
        }
        f32x4* sR = (f32x4*)smem;
#pragma unroll
        for (int nf = 0; nf < 4; ++nf)
#pragma unroll
            for (int mf = 0; mf < 2; ++mf) sR[(wv * 8 + nf * 2 + mf) * 64 + l] = acc[nf][mf];
        __syncthreads();
        f32x4 a0 = zero, a1 = zero;
#pragma unroll
        for (int ww = 0; ww < 4; ++ww) {
            a0 += sR[((mh * 4 + ww) * 8 + (2 * fp) * 2 + (kq >> 1)) * 64 + l];
            a1 += sR[((mh * 4 + ww) * 8 + (2 * fp + 1) * 2 + (kq >> 1)) * 64 + l];
        }
        u32x4 o;
        o[0] = pk_bf16(bf_lo(xv2[0]) + bf_lo(gv2[0]) * a0[0], bf_hi(xv2[0]) + bf_hi(gv2[0]) * a0[1]);
        o[1] = pk_bf16(bf_lo(xv2[1]) + bf_lo(gv2[1]) * a0[2], bf_hi(xv2[1]) + bf_hi(gv2[1]) * a0[3]);
        o[2] = pk_bf16(bf_lo(xv2[2]) + bf_lo(gv2[2]) * a1[0], bf_hi(xv2[2]) + bf_hi(gv2[2]) * a1[1]);
        o[3] = pk_bf16(bf_lo(xv2[3]) + bf_lo(gv2[3]) * a1[2], bf_hi(xv2[3]) + bf_hi(gv2[3]) * a1[3]);
        bst(rs, (unsigned)(OFF_X1 + (((size_t)(mt2 * 4 + fmf) * 16 + (nt2 * 2 + fp)) * 64 + l) * 16), o);
        float vals[8];
#pragma unroll
        for (int i = 0; i < 4; ++i) { vals[2 * i] = bf_lo(o[i]); vals[2 * i + 1] = bf_hi(o[i]); }
        float s = ((vals[0] + vals[1]) + (vals[2] + vals[3])) + ((vals[4] + vals[5]) + (vals[6] + vals[7])), m, dm;
        red2_add(s, 0.f, m, dm);
        m *= (1.f / 32.f);
        float q = 0.f, Q;
#pragma unroll
        for (int i = 0; i < 8; ++i) { const float d = vals[i] - m; q += d * d; }
        red2_add(q, 0.f, Q, dm);
        if (g == 0) bst64(rs, (unsigned)(OFF_ST + (((size_t)(mt2 * 4 + fmf) * 16 + nt2 * 2 + fp) * 16 + (l & 15)) * 8), (u32x2){f2u(m), f2u(Q)});
        DRAIN();
        __syncthreads();
        if (threadIdx.x == 0) set_flag(cnt + (CNT_CX + mt2) * 64 + nt2);
    }
    if (wv == 7) {
#pragma unroll
        for (int i = 0; i < B3_K; ++i) K3_LOADB(i);
    }
#pragma unroll
    for (int i = B3_K; i < B3_A; ++i) K3_LOADB(i);
    const unsigned wdoff = (unsigned)(OFF_WD + (((size_t)nt2 * 4 * KS_F + kq) * 64 + l) * 16);
    u32x4 wdf[11][4];
    const int last4 = kq < 3 ? 40 : 36;
#define K4_LOADW(j) do { const int i_ = (j) >> 2, f_ = (j) & 3; wdf[i_][f_] = bld(rs, wdoff + (f_ * KS_F + (i_ < 10 ? 4 * i_ : last4)) * 1024); } while (0)
    if (act3) {
        u32x4* sA = smem;
        if (wv == 6) {
            const int ma = (mf0 * 16) >> 6, mb = (mf0 * 16 + nmf * 16 - 1) >> 6;
            wait_cnt(cnt + (CNT_CX + ma) * 64, 8u);
            wait_cnt(cnt + (CNT_CX + mb) * 64, 8u);
        }
        __syncthreads();
        u32x4 xa[4][3];
        u32x2 stp[4];
#define K3_LDA(c) do { _Pragma("unroll") for (int i = 0; i < 3; ++i) { const int mf = 3 * mh + i, mfa = mf0 + (mf < nmf ? mf : 0); \
                xa[c][i] = bld(rs, (unsigned)(OFF_X1 + (((size_t)mfa * 16 + 4 * (c) + kq) * 64 + l) * 16)); } SB(); } while (0)
#define K3_WRA(c) do { _Pragma("unroll") for (int i = 0; i < 3; ++i) sA[((3 * mh + i) * 16 + 4 * (c) + kq) * 64 + l] = xa[c][i]; SB(); } while (0)
#define K3_RDA(buf, ks) do { _Pragma("unroll") for (int mf = 0; mf < 6; ++mf) af[buf][mf] = sA[(mf * 16 + (ks)) * 64 + l]; } while (0)
        K3_LDA(0); K3_LDA(1); K3_LDA(2); K3_LDA(3);
#pragma unroll
        for (int i = B3_A; i < 16; ++i) K3_LOADB(i);
        K3_WRA(0); K3_WRA(1); K3_WRA(2); K3_WRA(3);
        __syncthreads(); SB();
        f32x4 ag[6], au[6];
#pragma unroll
        for (int mf = 0; mf < 6; ++mf) { ag[mf] = zero; au[mf] = zero; }
        u32x4 af[2][6];
        f32x4 cpart;
        K3_RDA(0, 0);
#pragma unroll
        for (int ks = 0; ks < 16; ++ks) {
            if (ks == 2) {
                const int i = wv < 6 ? wv : 0, mfa = mf0 + (i < nmf ? i : 0);
#pragma unroll
                for (int j = 0; j < 4; ++j) stp[j] = bld64(rs, (unsigned)(OFF_ST + (((size_t)mfa * 16 + 4 * g + j) * 16 + (l & 15)) * 8));
                SB();
            }
            if (ks == 9) {
                float m, dummy, q;
                const float m0 = u2f(stp[0][0]), m1 = u2f(stp[1][0]), m2 = u2f(stp[2][0]), m3 = u2f(stp[3][0]);
                red2_add((m0 + m1) + (m2 + m3), 0.f, m, dummy);
                m *= 0.0625f;
                const float e0 = m0 - m, e1 = m1 - m, e2 = m2 - m, e3 = m3 - m;
                red2_add(((u2f(stp[0][1]) + u2f(stp[1][1])) + (u2f(stp[2][1]) + u2f(stp[3][1]))) + 32.f * ((e0 * e0 + e1 * e1) + (e2 * e2 + e3 * e3)), 0.f, q, dummy);
                if (g == 0 && wv < 6) sSt[wv * 16 + l] = (f32x2){rsqrtf(q * (1.f / D_) + 1e-5f), -m};
                SB();
            }
            if (ks == 11) { __syncthreads(); SB(); }
            if (ks == 12) {
                const unsigned co = (unsigned)(OFF_C12 + (size_t)(actw ? pair : ng * 8) * 1024 + (l & 15) * 64 + g * 16);
                cpart = __builtin_bit_cast(f32x4, bld(rs, co)); SB();
            }
            if (ks + 1 < 16) K3_RDA((ks + 1) & 1, ks + 1);
            SB();
#pragma unroll
            for (int mf = 0; mf < 6; ++mf) { ag[mf] = MFMA32(bg[ks], af[ks & 1][mf], ag[mf]); au[mf] = MFMA32(bu[ks], af[ks & 1][mf], au[mf]); }
            SB();
            if (ks >= 3 && ks < 15) { K4_LOADW(2 * (ks - 3)); K4_LOADW(2 * (ks - 3) + 1); SB(); }
        }
        f32x4 c1g, c2g, c1u, c2u;
#pragma unroll
        for (int j = 0; j < 4; ++j) {
            float v = cpart[j];
            v += u2f(__builtin_amdgcn_update_dpp(0, f2u(v), 0x124, 0xf, 0xf, false));
            v += u2f(__builtin_amdgcn_update_dpp(0, f2u(v), 0x128, 0xf, 0xf, false));
            c1g[j] = u2f(__builtin_amdgcn_update_dpp(0, f2u(v), 0x00, 0xf, 0xf, false));
            c2g[j] = u2f(__builtin_amdgcn_update_dpp(0, f2u(v), 0x55, 0xf, 0xf, false));
            c1u[j] = u2f(__builtin_amdgcn_update_dpp(0, f2u(v), 0xaa, 0xf, 0xf, false));
            c2u[j] = u2f(__builtin_amdgcn_update_dpp(0, f2u(v), 0xff, 0xf, 0xf, false));
        }
        if (actw) {
#pragma unroll
            for (int mp = 0; mp < 3; ++mp) {
                if (2 * mp < nmf) {
                    u32x4 ov;
#pragma unroll
                    for (int q = 0; q < 2; ++q) {
                        const int mf = 2 * mp + q;
                        const f32x2 st = sSt[mf * 16 + (l & 15)];
                        float hv[4];
#pragma unroll
                        for (int j = 0; j < 4; ++j) {
                            const float gq = (ag[mf][j] + st[1] * c1g[j]) * st[0] + c2g[j];
                            const float uq = (au[mf][j] + st[1] * c1u[j]) * st[0] + c2u[j];
                            hv[j] = gelu_tanh(gq) * uq;
                        }
                        ov[2 * q] = pk_bf16(hv[0], hv[1]); ov[2 * q + 1] = pk_bf16(hv[2], hv[3]);
                    }
                    bst(rs, (unsigned)(OFF_HH + (((size_t)((mf0 >> 1) + mp) * NPAIR + pair) * 64 + l) * 16), ov);
                }
            }
        }
        DRAIN();
        __syncthreads();
        if (threadIdx.x == 0) set_flag(cnt + (CNT_CH + mt3) * 64 + ng);
    } else {
#pragma unroll
        for (int j = 0; j < 24; ++j) K4_LOADW(j);
    }
#pragma unroll
    for (int j = 24; j < 44; ++j) K4_LOADW(j);
    SB();
    {
        if (wv == 7) {
            const int ma = (mt2 * 64) / 96, mb = (mt2 * 64 + 63) / 96;
            wait_cnt(cnt + (CNT_CH + ma) * 64, 11u);
            wait_cnt(cnt + (CNT_CH + mb) * 64, 11u);
        }
        __syncthreads();
        f32x4 acc[4][2];
#pragma unroll
        for (int a = 0; a < 4; ++a) { acc[a][0] = zero; acc[a][1] = zero; }
        u32x4 hf[11][2];
#define K4_LOADH(i) do { const unsigned o_ = (unsigned)(OFF_HH + (((size_t)(mt2 * 2 + mh) * NPAIR + 2 * (kq + ((i) < 10 ? 4 * (i) : last4))) * 64 + l) * 16); \
            hf[i][0] = bld(rs, o_); hf[i][1] = bld(rs, o_ + 1024); SB(); } while (0)
#pragma unroll
        for (int i = 0; i < 3; ++i) K4_LOADH(i);
        const u32x4 xv = bld(rs, (unsigned)(OFF_X1 + (((size_t)(mt2 * 4 + fmf) * 16 + (nt2 * 2 + fp)) * 64 + l) * 16));
        const u32x4 gv = *(const u32x4*)((const u16*)(ws + OFF_MOD) + 2560 + n02);
        SB();
#pragma unroll
        for (int i = 0; i < 11; ++i) {
            if (i + 3 < 11) K4_LOADH(i + 3);
            if (i < 10 || kq < 3) {
                const u32x4 h0 = {hf[i][0][0], hf[i][0][1], hf[i][1][0], hf[i][1][1]}, h1 = {hf[i][0][2], hf[i][0][3], hf[i][1][2], hf[i][1][3]};
#pragma unroll
                for (int nf = 0; nf < 4; ++nf) { acc[nf][0] = MFMA32(wdf[i][nf], h0, acc[nf][0]); acc[nf][1] = MFMA32(wdf[i][nf], h1, acc[nf][1]); }
            }
            SB();
        }
        f32x4* sR = (f32x4*)smem;
#pragma unroll
        for (int nf = 0; nf < 4; ++nf)
#pragma unroll
            for (int mf = 0; mf < 2; ++mf) sR[(wv * 8 + nf * 2 + mf) * 64 + l] = acc[nf][mf];
        __syncthreads();
        f32x4 a0 = zero, a1 = zero;
#pragma unroll
        for (int ww = 0; ww < 4; ++ww) {
            a0 += sR[((mh * 4 + ww) * 8 + (2 * fp) * 2 + (kq >> 1)) * 64 + l];
            a1 += sR[((mh * 4 + ww) * 8 + (2 * fp + 1) * 2 + (kq >> 1)) * 64 + l];
        }
        u32x4 o;
        o[0] = pk_bf16(bf_lo(xv[0]) + bf_lo(gv[0]) * a0[0], bf_hi(xv[0]) + bf_hi(gv[0]) * a0[1]);
        o[1] = pk_bf16(bf_lo(xv[1]) + bf_lo(gv[1]) * a0[2], bf_hi(xv[1]) + bf_hi(gv[1]) * a0[3]);
        o[2] = pk_bf16(bf_lo(xv[2]) + bf_lo(gv[2]) * a1[0], bf_hi(xv[2]) + bf_hi(gv[2]) * a1[1]);
        o[3] = pk_bf16(bf_lo(xv[3]) + bf_lo(gv[3]) * a1[2], bf_hi(xv[3]) + bf_hi(gv[3]) * a1[3]);
        *(u32x4*)(out + (size_t)tok2 * D_ + n02) = o;
    }

}

extern "C" void kernel_launch(void* const* d_in, const int* in_sizes, int n_in,
                              void* d_out, int out_size, void* d_ws, size_t ws_size,
                              hipStream_t stream) {
    const u16* x = (const u16*)d_in[0];
    const u16* wqkv = (const u16*)d_in[1];
    const u16* wphi = (const u16*)d_in[2];
    const u16* wo = (const u16*)d_in[3];
    const u16* wg = (const u16*)d_in[5];
    const u16* wu = (const u16*)d_in[6];
    const u16* wd = (const u16*)d_in[7];
    const u16* cvec = (const u16*)d_in[8];
    const u16* wmod = (const u16*)d_in[9];
    char* ws = (char*)d_ws;
    k0_prep<<<K0_GRID, 256, 0, stream>>>(x, wqkv, wphi, cvec, wmod, wo, ws);
    kf_main<<<256, 512, 0, stream>>>(x, wg, wu, wd, ws, (u16*)d_out);
}
```
